```python
import torch

def _softmax(x, axis=-1):
    return torch.softmax(x, dim=axis)
def _gelu(x):
    return torch.nn.functional.gelu(x, approximate="tanh")
def _silu(x):
    return torch.nn.functional.silu(x)
def _sigmoid(x):
    return torch.sigmoid(x)
def _layernorm(x, axis=-1, eps=1e-5):
    m = x.mean(dim=axis, keepdim=True)
    v = x.var(dim=axis, keepdim=True, unbiased=False)
    return (x - m) / torch.sqrt(v + eps)
def _scatter_add(tgt, idx, src, axis=0):
    return tgt.clone().index_add_(axis, idx, src)
def _segment_sum(src, seg, n):
    out = torch.zeros((n,) + src.shape[1:], dtype=src.dtype)
    return out.index_add_(0, seg, src)
def _pad(x, pw):
    flat = tuple(v for pair in reversed(pw) for v in pair)
    return torch.nn.functional.pad(x, flat)

kernel_name = 'recurrent_scan__sliding_block_cached__geglu_ffn__decode'

def setup_inputs():

    _w = (lambda a: (lambda t: t.to(torch.bfloat16) if t.is_floating_point() else t)(torch.as_tensor(a)))
    def _A_setup():
        B, Sq, D = 1, 1, 512
        Hq, Hkv, Sk, Dh = 32, 8, 32768, 128
        assert Hq % Hkv == 0, (Hq, Hkv)
        return {
            "x": _w((lambda *s: torch.randn(*s, dtype=torch.bfloat16) * 0.05)(B, Sq, D)),
            "k_cache": _w((lambda *s: torch.randn(*s, dtype=torch.bfloat16) * 1.6)(B, Hkv, Sk, Dh)),
            "v_cache": _w((lambda *s: torch.randn(*s, dtype=torch.bfloat16) * 0.2)(B, Hkv, Sk, Dh)),
            "wq": _w((lambda *s: torch.randn(*s, dtype=torch.bfloat16) * 1.6)(D, Hq * Dh) * (D ** -0.5)),
            "wo": _w((lambda *s: torch.randn(*s, dtype=torch.bfloat16) * 0.2)(Hq * Dh, D) * ((Hq * Dh) ** -0.5)),
        }
    _A_in = _A_setup()

    def _B_setup():
        B, S, D, F = 1, 1, 512, 1792
        return {
            "x": _w((lambda *s: torch.randn(*s, dtype=torch.bfloat16) * 0.05)(B, S, D)),
            "w_gate": _w((lambda *s: torch.randn(*s, dtype=torch.bfloat16) * 0.1)(D, F)),
            "w_up": _w((lambda *s: torch.randn(*s, dtype=torch.bfloat16) * 0.1)(D, F)),
            "w_down": _w((lambda *s: torch.randn(*s, dtype=torch.bfloat16) * 0.1)(F, D)),
        }
    _B_in = _B_setup()

    return {
        "A_x": _A_in["x"],
        "A_k_cache": _A_in["k_cache"],
        "A_v_cache": _A_in["v_cache"],
        "A_wq": _A_in["wq"],
        "A_wo": _A_in["wo"],
        "B_x": _B_in["x"],
        "B_w_gate": _B_in["w_gate"],
        "B_w_up": _B_in["w_up"],
        "B_w_down": _B_in["w_down"],
    }

def reference(A_x, A_k_cache, A_v_cache, A_wq, A_wo, B_x, B_w_gate, B_w_up, B_w_down):
    def _A_ref(x, k_cache, v_cache, wq, wo):
        B, Sq, D = x.shape
        Hq, Dh = 32, 128
        Hkv, Sk = k_cache.shape[1], k_cache.shape[2]
        W = min(4096, max(256, Sk // 4))
        rep = Hq // Hkv
        q = (lambda x, *d: x.permute(*d))((x @ wq).reshape(B, Sq, Hq, Dh), 0, 2, 1, 3)
        k = (lambda x, n, axis: torch.repeat_interleave(x, n, dim=axis))(k_cache[:, :, Sk - W:, :], rep, axis=1)
        v = (lambda x, n, axis: torch.repeat_interleave(x, n, dim=axis))(v_cache[:, :, Sk - W:, :], rep, axis=1)
        scores = torch.einsum("bhqd,bhkd->bhqk", q, k) * (Dh ** -0.5)
        p = _softmax(scores, axis=-1)
        o = torch.einsum("bhqk,bhkd->bhqd", p, v)
        return (lambda x, *d: x.permute(*d))(o, 0, 2, 1, 3).reshape(B, Sq, Hq * Dh) @ wo

    def _B_ref(x, w_gate, w_up, w_down):
        return (_gelu(x @ w_gate) * (x @ w_up)) @ w_down

    x = A_x
    xn = _layernorm(x, axis=-1)
    s = _A_ref(xn, A_k_cache, A_v_cache, A_wq, A_wo)
    g = _silu(_B_ref(xn, B_w_gate, B_w_up, B_w_down))
    return x + s * g
```

```cpp
#include <hip/hip_runtime.h>
#include <hip/hip_bf16.h>
#include <stdint.h>

typedef unsigned short u16;
__device__ __forceinline__ float bf2f(u16 b) { return __uint_as_float((unsigned)b << 16); }
__device__ __forceinline__ float rbf(float f) {
  unsigned u = __float_as_uint(f); u += 0x7fffu + ((u >> 16) & 1u); return __uint_as_float(u & 0xffff0000u);
}
constexpr int D = 512, HQ = 32, HKV = 8, SK = 32768, DH = 128, W = 4096, F = 1792;
constexpr int NSPLIT = 32, KPS = W / NSPLIT;

__device__ void layernorm_lds(const u16* x, float* xn, float* red) {
  int t = threadIdx.x;
  float a = bf2f(x[t]), b = bf2f(x[t + 256]);
  float s = a + b;
  for (int o = 32; o; o >>= 1) s += __shfl_xor(s, o);
  if ((t & 63) == 0) red[t >> 6] = s;
  __syncthreads();
  float mean = (red[0] + red[1] + red[2] + red[3]) * (1.0f / 512.0f);
  __syncthreads();
  float da = a - mean, db = b - mean;
  float v = da * da + db * db;
  for (int o = 32; o; o >>= 1) v += __shfl_xor(v, o);
  if ((t & 63) == 0) red[t >> 6] = v;
  __syncthreads();
  float var = (red[0] + red[1] + red[2] + red[3]) * (1.0f / 512.0f);
  float mb = rbf(mean), vb = rbf(var);
  float sd = rbf(sqrtf(rbf(vb + 1e-5f)));
  xn[t] = rbf(rbf(a - mb) / sd);
  xn[t + 256] = rbf(rbf(b - mb) / sd);
  __syncthreads();
}

__global__ void k1(const u16* x, const u16* wq, const u16* wg, const u16* wu, float* q, float* h) {
  __shared__ float xn[512]; __shared__ float red[4];
  layernorm_lds(x, xn, red);
  int c = blockIdx.x * 256 + threadIdx.x;
  if (c < 4096) {
    float acc = 0;
    for (int k = 0; k < D; ++k) acc += xn[k] * bf2f(wq[(size_t)k * 4096 + c]);
    q[c] = rbf(acc);
  } else {
    c -= 4096;
    if (c < F) {
      float g = 0, u = 0;
      for (int k = 0; k < D; ++k) { g += xn[k] * bf2f(wg[(size_t)k * F + c]); u += xn[k] * bf2f(wu[(size_t)k * F + c]); }
      g = rbf(g); u = rbf(u);
      float ge = 0.5f * g * (1.0f + tanhf(0.7978845608028654f * (g + 0.044715f * g * g * g)));
      h[c] = rbf(rbf(ge) * u);
    }
  }
}
__global__ void k2(const u16* kc, const u16* vc, const float* q, float* po, float* pml) {
  __shared__ float p[KPS]; __shared__ float red[2];
  int s = blockIdx.x, h = blockIdx.y, g = h / 4, t = threadIdx.x;
  const u16* kb = kc + ((size_t)g * SK + (SK - W) + s * KPS) * DH;
  const u16* vb = vc + ((size_t)g * SK + (SK - W) + s * KPS) * DH;
  float acc = 0;
  for (int d = 0; d < DH; ++d) acc += q[h * DH + d] * bf2f(kb[t * DH + d]);
  float sc = rbf(rbf(acc) * 0.08838834764831845f);
  float m = sc;
  for (int o = 32; o; o >>= 1) m = fmaxf(m, __shfl_xor(m, o));
  if ((t & 63) == 0) red[t >> 6] = m;
  __syncthreads();
  m = fmaxf(red[0], red[1]);
  __syncthreads();
  float e = expf(sc - m);
  p[t] = e;
  float l = e;
  for (int o = 32; o; o >>= 1) l += __shfl_xor(l, o);
  if ((t & 63) == 0) red[t >> 6] = l;
  __syncthreads();
  l = red[0] + red[1];
  float o = 0;
  for (int j = 0; j < KPS; ++j) o += p[j] * bf2f(vb[j * DH + t]);
  po[((size_t)h * NSPLIT + s) * DH + t] = o;
  if (t == 0) { pml[(h * NSPLIT + s) * 2] = m; pml[(h * NSPLIT + s) * 2 + 1] = l; }
}
__global__ void k3(const float* po, const float* pml, float* o) {
  int h = blockIdx.x, t = threadIdx.x;
  float M = -INFINITY;
  for (int s = 0; s < NSPLIT; ++s) M = fmaxf(M, pml[(h * NSPLIT + s) * 2]);
  float L = 0, a = 0;
  for (int s = 0; s < NSPLIT; ++s) { float w = expf(pml[(h * NSPLIT + s) * 2] - M); L += w * pml[(h * NSPLIT + s) * 2 + 1]; a += w * po[((size_t)h * NSPLIT + s) * DH + t]; }
  o[h * DH + t] = rbf(a / L);
}
__global__ void k4(const u16* x, const float* o, const float* h, const u16* wo, const u16* wd, u16* out) {
  int c = blockIdx.x * 64 + threadIdx.x;
  float s = 0, f = 0;
  for (int k = 0; k < 4096; ++k) s += o[k] * bf2f(wo[(size_t)k * D + c]);
  for (int k = 0; k < F; ++k) f += h[k] * bf2f(wd[(size_t)k * D + c]);
  s = rbf(s); f = rbf(f);
  float g = rbf(f / (1.0f + expf(-f)));
  float r = rbf(bf2f(x[c]) + rbf(s * g));
  out[c] = (u16)(__float_as_uint(r) >> 16);
}

extern "C" void kernel_launch(void* const* d_in, const int* in_sizes, int n_in,
                              void* d_out, int out_size, void* d_ws, size_t ws_size,
                              hipStream_t stream) {
  const u16* x = (const u16*)d_in[0];
  const u16* kc = (const u16*)d_in[1];
  const u16* vc = (const u16*)d_in[2];
  const u16* wq = (const u16*)d_in[3];
  const u16* wo = (const u16*)d_in[4];
  const u16* wg = (const u16*)d_in[6];
  const u16* wu = (const u16*)d_in[7];
  const u16* wd = (const u16*)d_in[8];
  float* ws = (float*)d_ws;
  float* q = ws; float* h = ws + 4096; float* o = ws + 8192; float* pml = ws + 16384; float* po = ws + 32768;
  k1<<<(4096 + F + 255) / 256, 256, 0, stream>>>(x, wq, wg, wu, q, h);
  k2<<<dim3(NSPLIT, HQ), 128, 0, stream>>>(kc, vc, q, po, pml);
  k3<<<32, 128, 0, stream>>>(po, pml, o);
  k4<<<8, 64, 0, stream>>>(x, o, h, wo, wd, (u16*)d_out);
}
```

```cpp
#include <hip/hip_runtime.h>
#include <stdint.h>

typedef unsigned u32;
typedef unsigned long long u64;
typedef u32 v4u __attribute__((ext_vector_type(4)));
typedef u32 v2u __attribute__((ext_vector_type(2)));
typedef float v4f __attribute__((ext_vector_type(4)));
typedef short v4s __attribute__((ext_vector_type(4)));
typedef __bf16 v8bf __attribute__((ext_vector_type(8)));
#define LAS __attribute__((address_space(3)))

#define WIN 22
#define NTW " nt"

constexpr int L_WQ = 0, L_GU = 16384, L_K = 32768, L_V = 65536, L_WD = 98304, L_WO = 106496;
constexpr int L_XN = 122880;
constexpr int L_Q = L_XN + 5 * 1024;
constexpr int L_P = L_Q + 1280;
constexpr int L_OC = L_P + 1280;
constexpr int OC_STR = 272;
constexpr int L_MX = L_OC + 8 * OC_STR;
constexpr int L_LS = L_MX + 64;
constexpr int L_ST = L_LS + 64;
constexpr int L_H = L_ST + 64;
constexpr int L_FL = L_H + 128;
constexpr int L_END = L_FL + 64;
constexpr u32 W_EP = 0;
constexpr u32 W_QB = 4096;
constexpr u32 W_GB = W_QB + 131072;
constexpr u32 W_OB = W_GB + 131072;
constexpr u32 W_SB = W_OB + 524288;
constexpr u32 W_FB = W_SB + 131072;
constexpr u32 W_STAMP = W_FB + 131072;

constexpr int SPIN_G = 1 << 17, SPIN_L = 1 << 22;

#define STAMP(i)

__device__ __forceinline__ float asf(u32 x) { return __builtin_bit_cast(float, x); }
__device__ __forceinline__ u32 asu(float x) { return __builtin_bit_cast(u32, x); }
__device__ __forceinline__ float lo16(u32 x) { return asf(x << 16); }
__device__ __forceinline__ float hi16(u32 x) { return asf(x & 0xffff0000u); }
typedef __bf16 v2bf __attribute__((ext_vector_type(2)));
typedef float v2f __attribute__((ext_vector_type(2)));
__device__ __forceinline__ u32 pk(float a, float b) { v2f v = {a, b}; return __builtin_bit_cast(u32, __builtin_convertvector(v, v2bf)); }
__device__ __forceinline__ float rb(float a) { return lo16(pk(a, a)); }
template <int C> __device__ __forceinline__ float dpp(float x) { return asf(__builtin_amdgcn_update_dpp(0, asu(x), C, 0xf, 0xf, true)); }
__device__ __forceinline__ float sum16_32(float x) {
  auto s = __builtin_amdgcn_permlane16_swap(asu(x), asu(x), false, false); x = asf(s[0]) + asf(s[1]);
  auto t = __builtin_amdgcn_permlane32_swap(asu(x), asu(x), false, false); return asf(t[0]) + asf(t[1]);
}
__device__ __forceinline__ float max16_32(float x) {
  auto s = __builtin_amdgcn_permlane16_swap(asu(x), asu(x), false, false); x = fmaxf(asf(s[0]), asf(s[1]));
  auto t = __builtin_amdgcn_permlane32_swap(asu(x), asu(x), false, false); return fmaxf(asf(t[0]), asf(t[1]));
}
__device__ __forceinline__ float wave_sum(float x) {
  x += dpp<0xB1>(x); x += dpp<0x4E>(x); x += dpp<0x141>(x); x += dpp<0x140>(x);
  return sum16_32(x);
}
__device__ __forceinline__ v4f mfma(v4u a, v4u b, v4f c) {
  return __builtin_amdgcn_mfma_f32_16x16x32_bf16(__builtin_bit_cast(v8bf, a), __builtin_bit_cast(v8bf, b), c, 0, 0, 0);
}
__device__ __forceinline__ v2u trrd(u32 a) { return __builtin_bit_cast(v2u, __builtin_amdgcn_ds_read_tr16_b64_v4i16((LAS v4s*)(size_t)a)); }
__device__ __forceinline__ v4u ld128(u32 a) { return *(LAS v4u*)(size_t)a; }
__device__ __forceinline__ v2u ld64(u32 a) { return *(LAS v2u*)(size_t)a; }
__device__ __forceinline__ void cbar() { asm volatile("" ::: "memory"); }
__device__ __forceinline__ void spin_ge(u32 addr, u32 want) {
  for (int n = SPIN_L; n > 0; --n) {
    u32 v = *(volatile LAS u32*)(size_t)addr;
    if (__builtin_amdgcn_readfirstlane(v) >= want) break;
  }
  cbar();
}
__device__ __forceinline__ void arrive(u32 addr, int lane) {
  cbar();
  if (lane == 0) __hip_atomic_fetch_add((LAS u32*)(size_t)addr, 1u, __ATOMIC_RELAXED, __HIP_MEMORY_SCOPE_WORKGROUP);
  cbar();
}
__device__ __forceinline__ void st64_dev(const char* base, u32 off, u32 a, u32 b) {
  v2u v = {a, b};
  asm volatile("global_store_dwordx2 %0, %1, %2 sc1" :: "v"(off), "v"(v), "s"(base) : "memory");
}
__device__ __forceinline__ void st128_dev(const char* base, u32 off, v4u v) {
  asm volatile("global_store_dwordx4 %0, %1, %2 sc1\n\ts_nop 1" :: "v"(off), "v"(v), "s"(base) : "memory");
}
template <int STEP> __device__ __forceinline__ void ld4_dev(const char* base, u32 off, v4u& a, v4u& b, v4u& c, v4u& d) {
  if constexpr (STEP == 1024) {
    asm volatile("global_load_dwordx4 %0, %4, %5 sc1\n\tglobal_load_dwordx4 %1, %4, %5 offset:1024 sc1\n\t"
                 "global_load_dwordx4 %2, %4, %5 offset:2048 sc1\n\tglobal_load_dwordx4 %3, %4, %5 offset:3072 sc1\n\ts_waitcnt vmcnt(0)"
                 : "=&v"(a), "=&v"(b), "=&v"(c), "=&v"(d) : "v"(off), "s"(base) : "memory");
  } else {
    u32 o2 = off + 2 * STEP;
    asm volatile("global_load_dwordx4 %0, %4, %6 offset:-4096 sc1\n\tglobal_load_dwordx4 %1, %4, %6 sc1\n\t"
                 "global_load_dwordx4 %2, %5, %6 offset:-4096 sc1\n\tglobal_load_dwordx4 %3, %5, %6 sc1\n\ts_waitcnt vmcnt(0)"
                 : "=&v"(a), "=&v"(b), "=&v"(c), "=&v"(d) : "v"(off + 4096), "v"(o2 + 4096), "s"(base) : "memory");
    static_assert(STEP == 4096, "");
  }
}
#define DMA(voff, base, m, OFF) asm volatile("s_mov_b32 m0, %2\n\ts_nop 0\n\tglobal_load_lds_dwordx4 %0, %1 offset:" #OFF NTW :: "v"(voff), "s"(base), "s"(m) : "memory")
template <int N> __device__ __forceinline__ void vmwait() { asm volatile("s_waitcnt vmcnt(%0)" :: "n"(N) : "memory"); }

__device__ __forceinline__ void lnorm(v4u xv, u32 dst, int lane) {
  float f[8];
#pragma unroll
  for (int i = 0; i < 4; ++i) { f[2 * i] = lo16(xv[i]); f[2 * i + 1] = hi16(xv[i]); }
  float s = ((f[0] + f[1]) + (f[2] + f[3])) + ((f[4] + f[5]) + (f[6] + f[7]));
  const float mean = wave_sum(s) * (1.0f / 512.0f);
  float q = 0.f;
#pragma unroll
  for (int i = 0; i < 8; ++i) { const float d = f[i] - mean; q = fmaf(d, d, q); }
  const float var = wave_sum(q) * (1.0f / 512.0f);
  const float mb = rb(mean);
  const float sd = rb(__builtin_sqrtf(rb(rb(var) + 1e-5f)));
  const float inv = 1.0f / sd;
  v4u o;
#pragma unroll
  for (int i = 0; i < 4; ++i) { const u32 d = pk(f[2 * i] - mb, f[2 * i + 1] - mb); o[i] = pk(lo16(d) * inv, hi16(d) * inv); }
  *(LAS v4u*)(size_t)(dst + lane * 16) = o;
}
__device__ __forceinline__ v4u afrag(u32 a) { v2u x = ld64(a), y = ld64(a + 32); return (v4u){x[0], x[1], y[0], y[1]}; }
__device__ __forceinline__ v4u wfrag(u32 a, int half) { v2u x = trrd(a), y = trrd(a + half); return (v4u){x[0], x[1], y[0], y[1]}; }

template <int I, int N, int E> struct MS {};

__global__ void __launch_bounds__(384, 1)
fused_decode(const char* __restrict__ x, const char* __restrict__ kc, const char* __restrict__ vc, const char* __restrict__ wq,
             const char* __restrict__ wo, const char* __restrict__ wg, const char* __restrict__ wu, const char* __restrict__ wd,
             char* __restrict__ out, char* __restrict__ ws) {
  __shared__ __attribute__((aligned(1024))) char lds[L_END];
  const u32 LB = (u32)(size_t)(LAS char*)lds;
  const int lane = threadIdx.x & 63, wave = __builtin_amdgcn_readfirstlane(threadIdx.x >> 6);
  const int b = blockIdx.x, g = b & 7, j = b >> 3;
  const int h = lane >> 4, c16 = lane & 15;
  STAMP(0);
  const u32 FL = LB + L_FL;
  if (wave == 0) {
    const u32 swz = (u32)(((((lane & 7) >> 1) ^ (h & 3)) << 1) | (lane & 1)) * 16u;
    const u32 r8 = lane >> 3;
    int n = 0;
#define PACE() do { if (n >= WIN) vmwait<WIN - 1>(); \
      if (n == 15 + WIN || n == 31 + WIN || n == 63 + WIN || n == 95 + WIN) { \
        const u32 pv = n == 15 + WIN ? 1u : n == 31 + WIN ? 2u : n == 63 + WIN ? 3u : 4u; *(volatile LAS u32*)(size_t)FL = pv; STAMP(pv + 1); } ++n; } while (0)
    {
      const u32 vo = r8 * 8192u + swz;
      u64 base = (u64)wq + (u64)(j >> 3) * (128u * 8192u) + (u32)(g * 1024 + (j & 7) * 128);
      u32 m = LB + L_WQ;
#pragma unroll
      for (int i = 0; i < 16; ++i) { PACE(); DMA(vo, base, m, 0); base += 65536; m += 1024; }
    }
    if (lane < 16) *(volatile LAS u32*)(size_t)(FL + lane * 4) = 0u;
    asm volatile("s_waitcnt lgkmcnt(0)\n\ts_barrier" ::: "memory");
    STAMP(1);
    {
      const bool on = b < 224;
      const int t = on ? b : 0;
      const u32 vo = on ? r8 * 3584u + swz : 0u;
      u64 base = (u64)((t & 1) ? wu : wg) + (u64)((t >> 1) & 3) * (128u * 3584u) + (u32)((t >> 3) * 128);
      const u32 step = on ? 8u * 3584u : 0u;
      u32 m = LB + L_GU;
#pragma unroll
      for (int i = 0; i < 16; ++i) { PACE(); DMA(vo, base, m, 0); base += step; m += 1024; }
    }
    const u64 kvoff = ((u64)g * 32768u + 28672u + (u32)j * 128u) * 256u;
    {
      const u32 v0 = h * 256u + (u32)(c16 ^ h) * 16u;
      const u32 v1 = v0 ^ 64u, v2 = v0 ^ 128u, v3 = v0 ^ 192u;
      u64 base = (u64)kc + kvoff; u32 m = LB + L_K;
#pragma unroll
      for (int i = 0; i < 8; ++i) {
        PACE(); DMA(v0, base, m, 0); PACE(); DMA(v1, base, m, 1024); PACE(); DMA(v2, base, m, 2048); PACE(); DMA(v3, base, m, 3072);
        base += 4096; m += 4096;
      }
    }
    {
      const u32 v0 = h * 256u + (u32)((((c16 >> 1) ^ h) << 1) | (lane & 1)) * 16u;
      const u32 v1 = v0 ^ 128u;
      u64 base = (u64)vc + kvoff; u32 m = LB + L_V;
#pragma unroll
      for (int i = 0; i < 8; ++i) {
        PACE(); DMA(v0, base, m, 0); PACE(); DMA(v1, base, m, 1024); PACE(); DMA(v0, base, m, 2048); PACE(); DMA(v1, base, m, 3072);
        base += 4096; m += 4096;
      }
    }
    const int hb = 4 * g + (j >> 3);
    {
      const bool on = g < 7;
      const u32 vo = on ? r8 * 1024u + swz : 0u;
      u64 base = (u64)wd + (on ? (u32)(hb * 65536 + (j & 7) * 128) : 0u);
      const u32 step = on ? 8192u : 0u;
      u32 m = LB + L_WD;
#pragma unroll
      for (int i = 0; i < 8; ++i) { PACE(); DMA(vo, base, m, 0); base += step; m += 1024; }
    }
    {
      const u32 vo = r8 * 1024u + swz;
      u64 base = (u64)wo + (u32)(hb * 131072 + (j & 7) * 128);
      u32 m = LB + L_WO;
#pragma unroll
      for (int i = 0; i < 16; ++i) { PACE(); DMA(vo, base, m, 0); base += 8192; m += 1024; }
    }
    static_assert(95 + WIN < 120 && WIN > 16, "milestones");
    vmwait<16>(); *(volatile LAS u32*)(size_t)FL = 5u; STAMP(6);
    vmwait<0>(); *(volatile LAS u32*)(size_t)FL = 6u; STAMP(7);
    if (b < 32) {
      u32 ep, xw;
      asm volatile("global_load_dword %0, %2, %3 sc1\n\tglobal_load_dword %1, %4, %5\n\ts_waitcnt vmcnt(0)"
                   : "=&v"(ep), "=&v"(xw) : "v"(0u), "s"(ws), "v"((u32)(b * 32 + (lane & 7) * 4)), "s"(x) : "memory");
      u32 tag = __builtin_amdgcn_readfirstlane(ep) + 1u; if (tag == 0u) tag = 1u;
      v4u r0, r1, r2, r3;
      const u32 off = (u32)b * 4096u + lane * 16u;
      const bool dead = lane >= 32;
      for (int n2 = SPIN_G; n2 > 0; --n2) {
        ld4_dev<1024>(ws + W_FB, off, r0, r1, r2, r3);
        const bool ok = (r0[1] == tag) & (r0[3] == tag) & (r1[1] == tag) & (r1[3] == tag) & (r2[1] == tag) & (r2[3] == tag) & (dead | ((r3[1] == tag) & (r3[3] == tag)));
        if (__all(ok)) break;
      }
      float f0 = (asf(r0[0]) + asf(r1[0])) + (asf(r2[0]) + (dead ? 0.f : asf(r3[0])));
      float f1 = (asf(r0[2]) + asf(r1[2])) + (asf(r2[2]) + (dead ? 0.f : asf(r3[2])));
      f0 += dpp<0x128>(f0); f1 += dpp<0x128>(f1);
      f0 = rb(sum16_32(f0)); f1 = rb(sum16_32(f1));
      const float g0 = rb(f0 / (1.0f + __expf(-f0))), g1 = rb(f1 / (1.0f + __expf(-f1)));
      STAMP(8);
      for (int n2 = SPIN_G; n2 > 0; --n2) {
        ld4_dev<1024>(ws + W_SB, off, r0, r1, r2, r3);
        const bool ok = (r0[1] == tag) & (r0[3] == tag) & (r1[1] == tag) & (r1[3] == tag) & (r2[1] == tag) & (r2[3] == tag) & (r3[1] == tag) & (r3[3] == tag);
        if (__all(ok)) break;
      }
      STAMP(9);
      float s0 = (asf(r0[0]) + asf(r1[0])) + (asf(r2[0]) + asf(r3[0]));
      float s1 = (asf(r0[2]) + asf(r1[2])) + (asf(r2[2]) + asf(r3[2]));
      s0 += dpp<0x128>(s0); s1 += dpp<0x128>(s1);
      s0 = sum16_32(s0); s1 = sum16_32(s1);
      const u32 sb = pk(s0, s1);
      const u32 pr = pk(lo16(sb) * g0, hi16(sb) * g1);
      const u32 res = pk(lo16(xw) + lo16(pr), hi16(xw) + hi16(pr));
      if (lane < 8) *(u32*)(out + b * 32 + lane * 4) = res;
      if (b == 0 && lane == 0) __hip_atomic_store((u32*)(ws + W_EP), tag, __ATOMIC_RELAXED, __HIP_MEMORY_SCOPE_AGENT);
      STAMP(10);
    }
  } else {
    u32 ep; v4u xv;
    asm volatile("global_load_dwordx4 %0, %2, %3\n\tglobal_load_dword %1, %4, %5 sc1\n\ts_barrier\n\ts_waitcnt vmcnt(0)"
                 : "=&v"(xv), "=&v"(ep) : "v"((u32)lane * 16u), "s"(x), "v"(0u), "s"(ws) : "memory");
    STAMP(1);
    u32 tag = __builtin_amdgcn_readfirstlane(ep) + 1u; if (tag == 0u) tag = 1u;
    const u32 XN = LB + L_XN + (wave - 1) * 1024;
    lnorm(xv, XN, lane);
    const int q4 = c16 >> 2, p4 = lane & 3;
    const u32 trow = (u32)(4 * h + q4);
    const u32 tsw = (u32)((2 * h + (q4 >> 1)) & 3);
    const v4f z4 = {0.f, 0.f, 0.f, 0.f};
    if (wave <= 4) {
      const int w = wave - 1;
      const u32 tb = trow * 128u + (((u32)w ^ tsw) << 5) + p4 * 8u;
      {
        v4u xa[4];
#pragma unroll
        for (int c = 0; c < 4; ++c) xa[c] = afrag(XN + (u32)(j >> 3) * 256u + c * 64 + h * 8);
        STAMP(2);
        spin_ge(FL, 1);
        STAMP(3);
        v4f d = z4;
#pragma unroll
        for (int c = 0; c < 4; ++c) d = mfma(xa[c], wfrag(LB + L_WQ + tb + c * 4096, 2048), d);
        if (lane < 16) st64_dev(ws + W_QB, (u32)((g * 32 + j) * 512 + w * 128 + lane * 8), asu(d[0]), tag);
        STAMP(4);
      }
      {
        v4u r0, r1, r2, r3;
        const u32 off = (u32)(g * 16384 + w * 1024 + lane * 16);
        for (int n2 = SPIN_G; n2 > 0; --n2) {
          ld4_dev<4096>(ws + W_QB, off, r0, r1, r2, r3);
          const bool ok = (r0[1] == tag) & (r0[3] == tag) & (r1[1] == tag) & (r1[3] == tag) & (r2[1] == tag) & (r2[3] == tag) & (r3[1] == tag) & (r3[3] == tag);
          if (__all(ok)) break;
        }
        const float q0 = (asf(r0[0]) + asf(r1[0])) + (asf(r2[0]) + asf(r3[0]));
        const float q1 = (asf(r0[2]) + asf(r1[2])) + (asf(r2[2]) + asf(r3[2]));
        *(LAS u32*)(size_t)(LB + L_Q + w * 320 + lane * 4) = pk(q0, q1);
        arrive(FL + 4, lane);
        STAMP(5);
      }
      spin_ge(FL, 3);
      v4u kf[2][4];
      {
        const u32 kb = LB + L_K + (u32)(32 * w + c16) * 256u + (u32)(h ^ c16) * 16u;
#pragma unroll
        for (int c = 0; c < 4; ++c) { kf[0][c] = ld128(kb ^ (c * 64)); kf[1][c] = ld128((kb ^ (c * 64)) + 4096); }
      }
      spin_ge(FL + 4, 4);
      STAMP(6);
      float s[8];
      {
        v4f d0 = z4, d1 = z4;
#pragma unroll
        for (int c = 0; c < 4; ++c) {
          const v4u qf = ld128(LB + L_Q + (c16 & 3) * 320 + c * 64 + h * 16);
          d0 = mfma(kf[0][c], qf, d0); d1 = mfma(kf[1][c], qf, d1);
        }
        const float SC = 0.08838834764831845f;
#pragma unroll
        for (int r = 0; r < 4; r += 2) {
          u32 a = pk(d0[r], d0[r + 1]); a = pk(lo16(a) * SC, hi16(a) * SC); s[r] = lo16(a); s[r + 1] = hi16(a);
          u32 e = pk(d1[r], d1[r + 1]); e = pk(lo16(e) * SC, hi16(e) * SC); s[4 + r] = lo16(e); s[5 + r] = hi16(e);
        }
      }
      float m = fmaxf(fmaxf(fmaxf(s[0], s[1]), fmaxf(s[2], s[3])), fmaxf(fmaxf(s[4], s[5]), fmaxf(s[6], s[7])));
      m = max16_32(m);
      if (lane < 4) *(LAS float*)(size_t)(LB + L_MX + lane * 16 + w * 4) = m;
      arrive(FL + 4, lane);
      spin_ge(FL + 4, 8);
      const float LOG2E = 1.4426950408889634f;
      float M2;
      {
        const v4u mm = ld128(LB + L_MX + (c16 & 3) * 16);
        M2 = fmaxf(fmaxf(asf(mm[0]), asf(mm[1])), fmaxf(asf(mm[2]), asf(mm[3])));
        float p[8];
#pragma unroll
        for (int r = 0; r < 8; ++r) p[r] = __builtin_amdgcn_exp2f((s[r] - M2) * LOG2E);
        M2 *= LOG2E;
        float l = ((p[0] + p[1]) + (p[2] + p[3])) + ((p[4] + p[5]) + (p[6] + p[7]));
        l = sum16_32(l);
        if (c16 < 4) *(LAS v4u*)(size_t)(LB + L_P + c16 * 320 + w * 64 + h * 16) = (v4u){pk(p[0], p[1]), pk(p[2], p[3]), pk(p[4], p[5]), pk(p[6], p[7])};
        if (lane < 4) *(LAS float*)(size_t)(LB + L_LS + lane * 16 + w * 4) = l;
      }
      arrive(FL + 4, lane);
      spin_ge(FL + 4, 12);
      {
        v4u pf[4];
#pragma unroll
        for (int c = 0; c < 4; ++c) pf[c] = ld128(LB + L_P + (c16 & 3) * 320 + c * 64 + h * 16);
        const v4u ll = ld128(LB + L_LS + (c16 & 3) * 16);
        const float l = (asf(ll[0]) + asf(ll[1])) + (asf(ll[2]) + asf(ll[3]));
        const float rl = __builtin_amdgcn_rcpf(l);
        const float lse = M2 + __builtin_amdgcn_logf(l);
        STAMP(7);
        spin_ge(FL, 4);
        STAMP(8);
        const u32 vb = LB + L_V + trow * 256u + ((((u32)(2 * w)) ^ (trow & 7u)) << 5) + p4 * 8u;
        v4f d0 = z4, d1 = z4;
#pragma unroll
        for (int c = 0; c < 4; ++c) {
          d0 = mfma(wfrag(vb + c * 8192, 4096), pf[c], d0);
          d1 = mfma(wfrag((vb ^ 32u) + c * 8192, 4096), pf[c], d1);
        }
        const bool t1 = (c16 & 4) != 0;
        const u32 a = pk((t1 ? d1[0] : d0[0]) * rl, (t1 ? d1[1] : d0[1]) * rl);
        const u32 e = pk((t1 ? d1[2] : d0[2]) * rl, (t1 ? d1[3] : d0[3]) * rl);
        const v4u pkt = {a, e, asu(lse), a ^ e ^ asu(lse) ^ tag};
        if (c16 < 8) st128_dev(ws + W_OB, (u32)(((4 * g + (c16 & 3)) * 32 + j) * 512 + (8 * w + (c16 & 4) + h) * 16), pkt);
        STAMP(9);
      }
      const int hd = 4 * g + (j >> 3);
      {
        v4u r[4];
        const u32 off = (u32)(hd * 16384 + w * 4096 + lane * 16);
        for (int n2 = SPIN_G; n2 > 0; --n2) {
          ld4_dev<1024>(ws + W_OB, off, r[0], r[1], r[2], r[3]);
          bool ok = true;
#pragma unroll
          for (int i = 0; i < 4; ++i) ok &= (r[i][0] ^ r[i][1] ^ r[i][2] ^ r[i][3]) == tag;
          if (__all(ok)) break;
        }
        STAMP(10);
        const float mw = fmaxf(fmaxf(asf(r[0][2]), asf(r[1][2])), fmaxf(asf(r[2][2]), asf(r[3][2])));
        float a0 = 0.f, a1 = 0.f, a2 = 0.f, a3 = 0.f, lw = 0.f;
#pragma unroll
        for (int i = 0; i < 4; ++i) {
          const float e = __builtin_amdgcn_exp2f(asf(r[i][2]) - mw);
          a0 = fmaf(e, lo16(r[i][0]), a0); a1 = fmaf(e, hi16(r[i][0]), a1); a2 = fmaf(e, lo16(r[i][1]), a2); a3 = fmaf(e, hi16(r[i][1]), a3); lw += e;
        }
        const int row = 2 * w + (lane >> 5);
        *(LAS v2u*)(size_t)(LB + L_OC + row * OC_STR + (lane & 31) * 8) = (v2u){pk(a0, a1), pk(a2, a3)};
        if ((lane & 31) == 0) *(LAS v2u*)(size_t)(LB + L_ST + row * 8) = (v2u){asu(mw), asu(lw)};
        arrive(FL + 4, lane);
      }
      spin_ge(FL, 6);
      v4u wf[4];
#pragma unroll
      for (int c = 0; c < 4; ++c) wf[c] = wfrag(LB + L_WO + tb + c * 4096, 2048);
      spin_ge(FL + 4, 16);
      {
        v4f d = z4;
#pragma unroll
        for (int c = 0; c < 4; ++c) d = mfma(afrag(LB + L_OC + (c16 & 7) * OC_STR + c * 64 + h * 8), wf[c], d);
        const u32 so = LB + L_ST + (h & 1) * 32;
        const v4u u0 = ld128(so), u1 = ld128(so + 16), u2 = ld128(so ^ 32), u3 = ld128((so ^ 32) + 16);
        const float M = fmaxf(fmaxf(fmaxf(asf(u0[0]), asf(u0[2])), fmaxf(asf(u1[0]), asf(u1[2]))), fmaxf(fmaxf(asf(u2[0]), asf(u2[2])), fmaxf(asf(u3[0]), asf(u3[2]))));
        const float e0 = __builtin_amdgcn_exp2f(asf(u0[0]) - M), e1 = __builtin_amdgcn_exp2f(asf(u0[2]) - M);
        const float e2 = __builtin_amdgcn_exp2f(asf(u1[0]) - M), e3 = __builtin_amdgcn_exp2f(asf(u1[2]) - M);
        float L = (e0 * asf(u0[1]) + e1 * asf(u0[3])) + (e2 * asf(u1[1]) + e3 * asf(u1[3]));
        float sv = (e0 * d[0] + e1 * d[1]) + (e2 * d[2] + e3 * d[3]);
        auto t0 = __builtin_amdgcn_permlane16_swap(asu(L), asu(L), false, false); L = asf(t0[0]) + asf(t0[1]);
        auto t1 = __builtin_amdgcn_permlane16_swap(asu(sv), asu(sv), false, false); sv = asf(t1[0]) + asf(t1[1]);
        sv = sv * __builtin_amdgcn_rcpf(L);
        if (lane < 16) st64_dev(ws + W_SB, (u32)((((j & 7) * 4 + w) * 32 + hd) * 128 + lane * 8), asu(sv), tag);
        STAMP(11);
      }
    } else {
      const u32 tb = trow * 128u + (tsw << 5) + p4 * 8u;
      if (b < 224) {
        v4u xa[4];
#pragma unroll
        for (int c = 0; c < 4; ++c) xa[c] = afrag(XN + (u32)((b >> 1) & 3) * 256u + c * 64 + h * 8);
        spin_ge(FL, 2);
        STAMP(3);
        v4f d[4] = {z4, z4, z4, z4};
#pragma unroll
        for (int c = 0; c < 4; ++c)
#pragma unroll
          for (int t = 0; t < 4; ++t) d[t] = mfma(xa[c], wfrag(LB + L_GU + (tb ^ (t * 32)) + c * 4096, 2048), d[t]);
        const float v = h == 0 ? d[0][0] : h == 1 ? d[1][0] : h == 2 ? d[2][0] : d[3][0];
        st64_dev(ws + W_GB, (u32)(b * 512 + lane * 8), asu(v), tag);
        STAMP(4);
      }
      if (g < 7) {
        const int hb = 4 * g + (j >> 3);
        v4u r0, r1, r2, r3;
        const u32 off = (u32)(hb * 4096 + lane * 16);
        for (int n2 = SPIN_G; n2 > 0; --n2) {
          ld4_dev<1024>(ws + W_GB, off, r0, r1, r2, r3);
          const bool ok = (r0[1] == tag) & (r0[3] == tag) & (r1[1] == tag) & (r1[3] == tag) & (r2[1] == tag) & (r2[3] == tag) & (r3[1] == tag) & (r3[3] == tag);
          if (__all(ok)) break;
        }
        STAMP(5);
        const u32 sm = pk((asf(r0[0]) + asf(r1[0])) + (asf(r2[0]) + asf(r3[0])), (asf(r0[2]) + asf(r1[2])) + (asf(r2[2]) + asf(r3[2])));
        auto sw = __builtin_amdgcn_permlane32_swap(sm, sm, false, false);
        const u32 gt = sw[0], up = sw[1];
        float hv[2];
#pragma unroll
        for (int i = 0; i < 2; ++i) {
          const float gx = i ? hi16(gt) : lo16(gt), ux = i ? hi16(up) : lo16(up);
          const float in = 0.7978845608028654f * (gx + 0.044715f * gx * gx * gx);
          const float th = 1.0f - 2.0f / (__expf(2.0f * in) + 1.0f);
          hv[i] = rb(0.5f * gx * (1.0f + th)) * ux;
        }
        if (lane < 32) *(LAS u32*)(size_t)(LB + L_H + lane * 4) = pk(hv[0], hv[1]);
        cbar();
        const v4u ha0 = afrag(LB + L_H + h * 8), ha1 = afrag(LB + L_H + 64 + h * 8);
        spin_ge(FL, 5);
        STAMP(6);
        v4f d[4] = {z4, z4, z4, z4};
#pragma unroll
        for (int t = 0; t < 4; ++t) { d[t] = mfma(ha0, wfrag(LB + L_WD + (tb ^ (t * 32)), 2048), d[t]); d[t] = mfma(ha1, wfrag(LB + L_WD + (tb ^ (t * 32)) + 4096, 2048), d[t]); }
        const float v = h == 0 ? d[0][0] : h == 1 ? d[1][0] : h == 2 ? d[2][0] : d[3][0];
        st64_dev(ws + W_FB, (u32)((((j & 7) * 4 + h) * 32 + hb) * 128 + c16 * 8), asu(v), tag);
        STAMP(7);
      }
    }
  }
}

extern "C" void kernel_launch(void* const* d_in, const int* in_sizes, int n_in, void* d_out, int out_size, void* d_ws, size_t ws_size,
                              hipStream_t stream) {
  fused_decode<<<256, 384, 0, stream>>>((const char*)d_in[0], (const char*)d_in[1], (const char*)d_in[2], (const char*)d_in[3],
                                        (const char*)d_in[4], (const char*)d_in[6], (const char*)d_in[7], (const char*)d_in[8],
                                        (char*)d_out, (char*)d_ws);
}
```

```cpp
#define STAMPS
#include <cstdio>
#include <vector>
#include <algorithm>
#include <cstdlib>
#include <hip/hip_runtime.h>
#include <stdint.h>

typedef unsigned u32;
typedef unsigned long long u64;
typedef u32 v4u __attribute__((ext_vector_type(4)));
typedef u32 v2u __attribute__((ext_vector_type(2)));
typedef float v4f __attribute__((ext_vector_type(4)));
typedef short v4s __attribute__((ext_vector_type(4)));
typedef __bf16 v8bf __attribute__((ext_vector_type(8)));
#define LAS __attribute__((address_space(3)))

#define WIN 22
#define NTW " nt"

constexpr int L_WQ = 0, L_GU = 16384, L_K = 32768, L_V = 65536, L_WD = 98304, L_WO = 106496;
constexpr int L_XN = 122880;
constexpr int L_Q = L_XN + 5 * 1024;
constexpr int L_P = L_Q + 1280;
constexpr int L_OC = L_P + 1280;
constexpr int OC_STR = 272;
constexpr int L_MX = L_OC + 8 * OC_STR;
constexpr int L_LS = L_MX + 64;
constexpr int L_ST = L_LS + 64;
constexpr int L_H = L_ST + 64;
constexpr int L_FL = L_H + 128;
constexpr int L_END = L_FL + 64;
constexpr u32 W_EP = 0;
constexpr u32 W_QB = 4096;
constexpr u32 W_GB = W_QB + 131072;
constexpr u32 W_OB = W_GB + 131072;
constexpr u32 W_SB = W_OB + 524288;
constexpr u32 W_FB = W_SB + 131072;
constexpr u32 W_STAMP = W_FB + 131072;

constexpr int SPIN_G = 1 << 17, SPIN_L = 1 << 22;

#define STAMP(i) do { u64 t_; asm volatile("s_memrealtime %0\n\ts_waitcnt lgkmcnt(0)" : "=s"(t_) :: "memory"); stamp[i] = t_; } while (0)

__device__ __forceinline__ float asf(u32 x) { return __builtin_bit_cast(float, x); }
__device__ __forceinline__ u32 asu(float x) { return __builtin_bit_cast(u32, x); }
__device__ __forceinline__ float lo16(u32 x) { return asf(x << 16); }
__device__ __forceinline__ float hi16(u32 x) { return asf(x & 0xffff0000u); }
typedef __bf16 v2bf __attribute__((ext_vector_type(2)));
typedef float v2f __attribute__((ext_vector_type(2)));
__device__ __forceinline__ u32 pk(float a, float b) { v2f v = {a, b}; return __builtin_bit_cast(u32, __builtin_convertvector(v, v2bf)); }
__device__ __forceinline__ float rb(float a) { return lo16(pk(a, a)); }
template <int C> __device__ __forceinline__ float dpp(float x) { return asf(__builtin_amdgcn_update_dpp(0, asu(x), C, 0xf, 0xf, true)); }
__device__ __forceinline__ float sum16_32(float x) {
  auto s = __builtin_amdgcn_permlane16_swap(asu(x), asu(x), false, false); x = asf(s[0]) + asf(s[1]);
  auto t = __builtin_amdgcn_permlane32_swap(asu(x), asu(x), false, false); return asf(t[0]) + asf(t[1]);
}
__device__ __forceinline__ float max16_32(float x) {
  auto s = __builtin_amdgcn_permlane16_swap(asu(x), asu(x), false, false); x = fmaxf(asf(s[0]), asf(s[1]));
  auto t = __builtin_amdgcn_permlane32_swap(asu(x), asu(x), false, false); return fmaxf(asf(t[0]), asf(t[1]));
}
__device__ __forceinline__ float wave_sum(float x) {
  x += dpp<0xB1>(x); x += dpp<0x4E>(x); x += dpp<0x141>(x); x += dpp<0x140>(x);
  return sum16_32(x);
}
__device__ __forceinline__ v4f mfma(v4u a, v4u b, v4f c) {
  return __builtin_amdgcn_mfma_f32_16x16x32_bf16(__builtin_bit_cast(v8bf, a), __builtin_bit_cast(v8bf, b), c, 0, 0, 0);
}
__device__ __forceinline__ v2u trrd(u32 a) { return __builtin_bit_cast(v2u, __builtin_amdgcn_ds_read_tr16_b64_v4i16((LAS v4s*)(size_t)a)); }
__device__ __forceinline__ v4u ld128(u32 a) { return *(LAS v4u*)(size_t)a; }
__device__ __forceinline__ v2u ld64(u32 a) { return *(LAS v2u*)(size_t)a; }
__device__ __forceinline__ void cbar() { asm volatile("" ::: "memory"); }
__device__ __forceinline__ void spin_ge(u32 addr, u32 want) {
  for (int n = SPIN_L; n > 0; --n) {
    u32 v = *(volatile LAS u32*)(size_t)addr;
    if (__builtin_amdgcn_readfirstlane(v) >= want) break;
  }
  cbar();
}
__device__ __forceinline__ void arrive(u32 addr, int lane) {
  cbar();
  if (lane == 0) __hip_atomic_fetch_add((LAS u32*)(size_t)addr, 1u, __ATOMIC_RELAXED, __HIP_MEMORY_SCOPE_WORKGROUP);
  cbar();
}
__device__ __forceinline__ void st64_dev(const char* base, u32 off, u32 a, u32 b) {
  v2u v = {a, b};
  asm volatile("global_store_dwordx2 %0, %1, %2 sc1" :: "v"(off), "v"(v), "s"(base) : "memory");
}
__device__ __forceinline__ void st128_dev(const char* base, u32 off, v4u v) {
  asm volatile("global_store_dwordx4 %0, %1, %2 sc1\n\ts_nop 1" :: "v"(off), "v"(v), "s"(base) : "memory");
}
template <int STEP> __device__ __forceinline__ void ld4_dev(const char* base, u32 off, v4u& a, v4u& b, v4u& c, v4u& d) {
  if constexpr (STEP == 1024) {
    asm volatile("global_load_dwordx4 %0, %4, %5 sc1\n\tglobal_load_dwordx4 %1, %4, %5 offset:1024 sc1\n\t"
                 "global_load_dwordx4 %2, %4, %5 offset:2048 sc1\n\tglobal_load_dwordx4 %3, %4, %5 offset:3072 sc1\n\ts_waitcnt vmcnt(0)"
                 : "=&v"(a), "=&v"(b), "=&v"(c), "=&v"(d) : "v"(off), "s"(base) : "memory");
  } else {
    u32 o2 = off + 2 * STEP;
    asm volatile("global_load_dwordx4 %0, %4, %6 offset:-4096 sc1\n\tglobal_load_dwordx4 %1, %4, %6 sc1\n\t"
                 "global_load_dwordx4 %2, %5, %6 offset:-4096 sc1\n\tglobal_load_dwordx4 %3, %5, %6 sc1\n\ts_waitcnt vmcnt(0)"
                 : "=&v"(a), "=&v"(b), "=&v"(c), "=&v"(d) : "v"(off + 4096), "v"(o2 + 4096), "s"(base) : "memory");
    static_assert(STEP == 4096, "");
  }
}
#define DMA(voff, base, m, OFF) asm volatile("s_mov_b32 m0, %2\n\ts_nop 0\n\tglobal_load_lds_dwordx4 %0, %1 offset:" #OFF NTW :: "v"(voff), "s"(base), "s"(m) : "memory")
template <int N> __device__ __forceinline__ void vmwait() { asm volatile("s_waitcnt vmcnt(%0)" :: "n"(N) : "memory"); }

__device__ __forceinline__ void lnorm(v4u xv, u32 dst, int lane) {
  float f[8];
#pragma unroll
  for (int i = 0; i < 4; ++i) { f[2 * i] = lo16(xv[i]); f[2 * i + 1] = hi16(xv[i]); }
  float s = ((f[0] + f[1]) + (f[2] + f[3])) + ((f[4] + f[5]) + (f[6] + f[7]));
  const float mean = wave_sum(s) * (1.0f / 512.0f);
  float q = 0.f;
#pragma unroll
  for (int i = 0; i < 8; ++i) { const float d = f[i] - mean; q = fmaf(d, d, q); }
  const float var = wave_sum(q) * (1.0f / 512.0f);
  const float mb = rb(mean);
  const float sd = rb(__builtin_sqrtf(rb(rb(var) + 1e-5f)));
  const float inv = 1.0f / sd;
  v4u o;
#pragma unroll
  for (int i = 0; i < 4; ++i) { const u32 d = pk(f[2 * i] - mb, f[2 * i + 1] - mb); o[i] = pk(lo16(d) * inv, hi16(d) * inv); }
  *(LAS v4u*)(size_t)(dst + lane * 16) = o;
}
__device__ __forceinline__ v4u afrag(u32 a) { v2u x = ld64(a), y = ld64(a + 32); return (v4u){x[0], x[1], y[0], y[1]}; }
__device__ __forceinline__ v4u wfrag(u32 a, int half) { v2u x = trrd(a), y = trrd(a + half); return (v4u){x[0], x[1], y[0], y[1]}; }

template <int I, int N, int E> struct MS {};

__global__ void __launch_bounds__(384, 1)
fused_decode(const char* __restrict__ x, const char* __restrict__ kc, const char* __restrict__ vc, const char* __restrict__ wq,
             const char* __restrict__ wo, const char* __restrict__ wg, const char* __restrict__ wu, const char* __restrict__ wd,
             char* __restrict__ out, char* __restrict__ ws) {
  __shared__ __attribute__((aligned(1024))) char lds[L_END];
  const u32 LB = (u32)(size_t)(LAS char*)lds;
  const int lane = threadIdx.x & 63, wave = __builtin_amdgcn_readfirstlane(threadIdx.x >> 6);
  const int b = blockIdx.x, g = b & 7, j = b >> 3;
  const int h = lane >> 4, c16 = lane & 15;
  u64 stamp[16];
#pragma unroll
  for (int i = 0; i < 16; ++i) stamp[i] = 0;
  STAMP(0);
  const u32 FL = LB + L_FL;
  if (wave == 0) {
    const u32 swz = (u32)(((((lane & 7) >> 1) ^ (h & 3)) << 1) | (lane & 1)) * 16u;
    const u32 r8 = lane >> 3;
    int n = 0;
#define PACE() do { if (n >= WIN) vmwait<WIN - 1>(); \
      if (n == 15 + WIN || n == 31 + WIN || n == 63 + WIN || n == 95 + WIN) { \
        const u32 pv = n == 15 + WIN ? 1u : n == 31 + WIN ? 2u : n == 63 + WIN ? 3u : 4u; *(volatile LAS u32*)(size_t)FL = pv; STAMP(pv + 1); } ++n; } while (0)
    {
      const u32 vo = r8 * 8192u + swz;
      u64 base = (u64)wq + (u64)(j >> 3) * (128u * 8192u) + (u32)(g * 1024 + (j & 7) * 128);
      u32 m = LB + L_WQ;
#pragma unroll
      for (int i = 0; i < 16; ++i) { PACE(); DMA(vo, base, m, 0); base += 65536; m += 1024; }
    }
    if (lane < 16) *(volatile LAS u32*)(size_t)(FL + lane * 4) = 0u;
    asm volatile("s_waitcnt lgkmcnt(0)\n\ts_barrier" ::: "memory");
    STAMP(1);
    {
      const bool on = b < 224;
      const int t = on ? b : 0;
      const u32 vo = on ? r8 * 3584u + swz : 0u;
      u64 base = (u64)((t & 1) ? wu : wg) + (u64)((t >> 1) & 3) * (128u * 3584u) + (u32)((t >> 3) * 128);
      const u32 step = on ? 8u * 3584u : 0u;
      u32 m = LB + L_GU;
#pragma unroll
      for (int i = 0; i < 16; ++i) { PACE(); DMA(vo, base, m, 0); base += step; m += 1024; }
    }
    const u64 kvoff = ((u64)g * 32768u + 28672u + (u32)j * 128u) * 256u;
    {
      const u32 v0 = h * 256u + (u32)(c16 ^ h) * 16u;
      const u32 v1 = v0 ^ 64u, v2 = v0 ^ 128u, v3 = v0 ^ 192u;
      u64 base = (u64)kc + kvoff; u32 m = LB + L_K;
#pragma unroll
      for (int i = 0; i < 8; ++i) {
        PACE(); DMA(v0, base, m, 0); PACE(); DMA(v1, base, m, 1024); PACE(); DMA(v2, base, m, 2048); PACE(); DMA(v3, base, m, 3072);
        base += 4096; m += 4096;
      }
    }
    {
      const u32 v0 = h * 256u + (u32)((((c16 >> 1) ^ h) << 1) | (lane & 1)) * 16u;
      const u32 v1 = v0 ^ 128u;
      u64 base = (u64)vc + kvoff; u32 m = LB + L_V;
#pragma unroll
      for (int i = 0; i < 8; ++i) {
        PACE(); DMA(v0, base, m, 0); PACE(); DMA(v1, base, m, 1024); PACE(); DMA(v0, base, m, 2048); PACE(); DMA(v1, base, m, 3072);
        base += 4096; m += 4096;
      }
    }
    const int hb = 4 * g + (j >> 3);
    {
      const bool on = g < 7;
      const u32 vo = on ? r8 * 1024u + swz : 0u;
      u64 base = (u64)wd + (on ? (u32)(hb * 65536 + (j & 7) * 128) : 0u);
      const u32 step = on ? 8192u : 0u;
      u32 m = LB + L_WD;
#pragma unroll
      for (int i = 0; i < 8; ++i) { PACE(); DMA(vo, base, m, 0); base += step; m += 1024; }
    }
    {
      const u32 vo = r8 * 1024u + swz;
      u64 base = (u64)wo + (u32)(hb * 131072 + (j & 7) * 128);
      u32 m = LB + L_WO;
#pragma unroll
      for (int i = 0; i < 16; ++i) { PACE(); DMA(vo, base, m, 0); base += 8192; m += 1024; }
    }
    static_assert(95 + WIN < 120 && WIN > 16, "milestones");
    vmwait<16>(); *(volatile LAS u32*)(size_t)FL = 5u; STAMP(6);
    vmwait<0>(); *(volatile LAS u32*)(size_t)FL = 6u; STAMP(7);
    if (b < 32) {
      u32 ep, xw;
      asm volatile("global_load_dword %0, %2, %3 sc1\n\tglobal_load_dword %1, %4, %5\n\ts_waitcnt vmcnt(0)"
                   : "=&v"(ep), "=&v"(xw) : "v"(0u), "s"(ws), "v"((u32)(b * 32 + (lane & 7) * 4)), "s"(x) : "memory");
      u32 tag = __builtin_amdgcn_readfirstlane(ep) + 1u; if (tag == 0u) tag = 1u;
      v4u r0, r1, r2, r3;
      const u32 off = (u32)b * 4096u + lane * 16u;
      const bool dead = lane >= 32;
      for (int n2 = SPIN_G; n2 > 0; --n2) {
        ld4_dev<1024>(ws + W_FB, off, r0, r1, r2, r3);
        const bool ok = (r0[1] == tag) & (r0[3] == tag) & (r1[1] == tag) & (r1[3] == tag) & (r2[1] == tag) & (r2[3] == tag) & (dead | ((r3[1] == tag) & (r3[3] == tag)));
        if (__all(ok)) break;
      }
      float f0 = (asf(r0[0]) + asf(r1[0])) + (asf(r2[0]) + (dead ? 0.f : asf(r3[0])));
      float f1 = (asf(r0[2]) + asf(r1[2])) + (asf(r2[2]) + (dead ? 0.f : asf(r3[2])));
      f0 += dpp<0x128>(f0); f1 += dpp<0x128>(f1);
      f0 = rb(sum16_32(f0)); f1 = rb(sum16_32(f1));
      const float g0 = rb(f0 / (1.0f + __expf(-f0))), g1 = rb(f1 / (1.0f + __expf(-f1)));
      STAMP(8);
      for (int n2 = SPIN_G; n2 > 0; --n2) {
        ld4_dev<1024>(ws + W_SB, off, r0, r1, r2, r3);
        const bool ok = (r0[1] == tag) & (r0[3] == tag) & (r1[1] == tag) & (r1[3] == tag) & (r2[1] == tag) & (r2[3] == tag) & (r3[1] == tag) & (r3[3] == tag);
        if (__all(ok)) break;
      }
      STAMP(9);
      float s0 = (asf(r0[0]) + asf(r1[0])) + (asf(r2[0]) + asf(r3[0]));
      float s1 = (asf(r0[2]) + asf(r1[2])) + (asf(r2[2]) + asf(r3[2]));
      s0 += dpp<0x128>(s0); s1 += dpp<0x128>(s1);
      s0 = sum16_32(s0); s1 = sum16_32(s1);
      const u32 sb = pk(s0, s1);
      const u32 pr = pk(lo16(sb) * g0, hi16(sb) * g1);
      const u32 res = pk(lo16(xw) + lo16(pr), hi16(xw) + hi16(pr));
      if (lane < 8) *(u32*)(out + b * 32 + lane * 4) = res;
      if (b == 0 && lane == 0) __hip_atomic_store((u32*)(ws + W_EP), tag, __ATOMIC_RELAXED, __HIP_MEMORY_SCOPE_AGENT);
      STAMP(10);
    }
  } else {
    u32 ep; v4u xv;
    asm volatile("global_load_dwordx4 %0, %2, %3\n\tglobal_load_dword %1, %4, %5 sc1\n\ts_barrier\n\ts_waitcnt vmcnt(0)"
                 : "=&v"(xv), "=&v"(ep) : "v"((u32)lane * 16u), "s"(x), "v"(0u), "s"(ws) : "memory");
    STAMP(1);
    u32 tag = __builtin_amdgcn_readfirstlane(ep) + 1u; if (tag == 0u) tag = 1u;
    const u32 XN = LB + L_XN + (wave - 1) * 1024;
    lnorm(xv, XN, lane);
    const int q4 = c16 >> 2, p4 = lane & 3;
    const u32 trow = (u32)(4 * h + q4);
    const u32 tsw = (u32)((2 * h + (q4 >> 1)) & 3);
    const v4f z4 = {0.f, 0.f, 0.f, 0.f};
    if (wave <= 4) {
      const int w = wave - 1;
      const u32 tb = trow * 128u + (((u32)w ^ tsw) << 5) + p4 * 8u;
      {
        v4u xa[4];
#pragma unroll
        for (int c = 0; c < 4; ++c) xa[c] = afrag(XN + (u32)(j >> 3) * 256u + c * 64 + h * 8);
        STAMP(2);
        spin_ge(FL, 1);
        STAMP(3);
        v4f d = z4;
#pragma unroll
        for (int c = 0; c < 4; ++c) d = mfma(xa[c], wfrag(LB + L_WQ + tb + c * 4096, 2048), d);
        if (lane < 16) st64_dev(ws + W_QB, (u32)((g * 32 + j) * 512 + w * 128 + lane * 8), asu(d[0]), tag);
        STAMP(4);
      }
      {
        v4u r0, r1, r2, r3;
        const u32 off = (u32)(g * 16384 + w * 1024 + lane * 16);
        for (int n2 = SPIN_G; n2 > 0; --n2) {
          ld4_dev<4096>(ws + W_QB, off, r0, r1, r2, r3);
          const bool ok = (r0[1] == tag) & (r0[3] == tag) & (r1[1] == tag) & (r1[3] == tag) & (r2[1] == tag) & (r2[3] == tag) & (r3[1] == tag) & (r3[3] == tag);
          if (__all(ok)) break;
        }
        const float q0 = (asf(r0[0]) + asf(r1[0])) + (asf(r2[0]) + asf(r3[0]));
        const float q1 = (asf(r0[2]) + asf(r1[2])) + (asf(r2[2]) + asf(r3[2]));
        *(LAS u32*)(size_t)(LB + L_Q + w * 320 + lane * 4) = pk(q0, q1);
        arrive(FL + 4, lane);
        STAMP(5);
      }
      spin_ge(FL, 3);
      v4u kf[2][4];
      {
        const u32 kb = LB + L_K + (u32)(32 * w + c16) * 256u + (u32)(h ^ c16) * 16u;
#pragma unroll
        for (int c = 0; c < 4; ++c) { kf[0][c] = ld128(kb ^ (c * 64)); kf[1][c] = ld128((kb ^ (c * 64)) + 4096); }
      }
      spin_ge(FL + 4, 4);
      STAMP(6);
      float s[8];
      {
        v4f d0 = z4, d1 = z4;
#pragma unroll
        for (int c = 0; c < 4; ++c) {
          const v4u qf = ld128(LB + L_Q + (c16 & 3) * 320 + c * 64 + h * 16);
          d0 = mfma(kf[0][c], qf, d0); d1 = mfma(kf[1][c], qf, d1);
        }
        const float SC = 0.08838834764831845f;
#pragma unroll
        for (int r = 0; r < 4; r += 2) {
          u32 a = pk(d0[r], d0[r + 1]); a = pk(lo16(a) * SC, hi16(a) * SC); s[r] = lo16(a); s[r + 1] = hi16(a);
          u32 e = pk(d1[r], d1[r + 1]); e = pk(lo16(e) * SC, hi16(e) * SC); s[4 + r] = lo16(e); s[5 + r] = hi16(e);
        }
      }
      float m = fmaxf(fmaxf(fmaxf(s[0], s[1]), fmaxf(s[2], s[3])), fmaxf(fmaxf(s[4], s[5]), fmaxf(s[6], s[7])));
      m = max16_32(m);
      if (lane < 4) *(LAS float*)(size_t)(LB + L_MX + lane * 16 + w * 4) = m;
      arrive(FL + 4, lane);
      spin_ge(FL + 4, 8);
      const float LOG2E = 1.4426950408889634f;
      float M2;
      {
        const v4u mm = ld128(LB + L_MX + (c16 & 3) * 16);
        M2 = fmaxf(fmaxf(asf(mm[0]), asf(mm[1])), fmaxf(asf(mm[2]), asf(mm[3])));
        float p[8];
#pragma unroll
        for (int r = 0; r < 8; ++r) p[r] = __builtin_amdgcn_exp2f((s[r] - M2) * LOG2E);
        M2 *= LOG2E;
        float l = ((p[0] + p[1]) + (p[2] + p[3])) + ((p[4] + p[5]) + (p[6] + p[7]));
        l = sum16_32(l);
        if (c16 < 4) *(LAS v4u*)(size_t)(LB + L_P + c16 * 320 + w * 64 + h * 16) = (v4u){pk(p[0], p[1]), pk(p[2], p[3]), pk(p[4], p[5]), pk(p[6], p[7])};
        if (lane < 4) *(LAS float*)(size_t)(LB + L_LS + lane * 16 + w * 4) = l;
      }
      arrive(FL + 4, lane);
      spin_ge(FL + 4, 12);
      {
        v4u pf[4];
#pragma unroll
        for (int c = 0; c < 4; ++c) pf[c] = ld128(LB + L_P + (c16 & 3) * 320 + c * 64 + h * 16);
        const v4u ll = ld128(LB + L_LS + (c16 & 3) * 16);
        const float l = (asf(ll[0]) + asf(ll[1])) + (asf(ll[2]) + asf(ll[3]));
        const float rl = __builtin_amdgcn_rcpf(l);
        const float lse = M2 + __builtin_amdgcn_logf(l);
        STAMP(7);
        spin_ge(FL, 4);
        STAMP(8);
        const u32 vb = LB + L_V + trow * 256u + ((((u32)(2 * w)) ^ (trow & 7u)) << 5) + p4 * 8u;
        v4f d0 = z4, d1 = z4;
#pragma unroll
        for (int c = 0; c < 4; ++c) {
          d0 = mfma(wfrag(vb + c * 8192, 4096), pf[c], d0);
          d1 = mfma(wfrag((vb ^ 32u) + c * 8192, 4096), pf[c], d1);
        }
        const bool t1 = (c16 & 4) != 0;
        const u32 a = pk((t1 ? d1[0] : d0[0]) * rl, (t1 ? d1[1] : d0[1]) * rl);
        const u32 e = pk((t1 ? d1[2] : d0[2]) * rl, (t1 ? d1[3] : d0[3]) * rl);
        const v4u pkt = {a, e, asu(lse), a ^ e ^ asu(lse) ^ tag};
        if (c16 < 8) st128_dev(ws + W_OB, (u32)(((4 * g + (c16 & 3)) * 32 + j) * 512 + (8 * w + (c16 & 4) + h) * 16), pkt);
        STAMP(9);
      }
      const int hd = 4 * g + (j >> 3);
      {
        v4u r[4];
        const u32 off = (u32)(hd * 16384 + w * 4096 + lane * 16);
        for (int n2 = SPIN_G; n2 > 0; --n2) {
          ld4_dev<1024>(ws + W_OB, off, r[0], r[1], r[2], r[3]);
          bool ok = true;
#pragma unroll
          for (int i = 0; i < 4; ++i) ok &= (r[i][0] ^ r[i][1] ^ r[i][2] ^ r[i][3]) == tag;
          if (__all(ok)) break;
        }
        STAMP(10);
        const float mw = fmaxf(fmaxf(asf(r[0][2]), asf(r[1][2])), fmaxf(asf(r[2][2]), asf(r[3][2])));
        float a0 = 0.f, a1 = 0.f, a2 = 0.f, a3 = 0.f, lw = 0.f;
#pragma unroll
        for (int i = 0; i < 4; ++i) {
          const float e = __builtin_amdgcn_exp2f(asf(r[i][2]) - mw);
          a0 = fmaf(e, lo16(r[i][0]), a0); a1 = fmaf(e, hi16(r[i][0]), a1); a2 = fmaf(e, lo16(r[i][1]), a2); a3 = fmaf(e, hi16(r[i][1]), a3); lw += e;
        }
        const int row = 2 * w + (lane >> 5);
        *(LAS v2u*)(size_t)(LB + L_OC + row * OC_STR + (lane & 31) * 8) = (v2u){pk(a0, a1), pk(a2, a3)};
        if ((lane & 31) == 0) *(LAS v2u*)(size_t)(LB + L_ST + row * 8) = (v2u){asu(mw), asu(lw)};
        arrive(FL + 4, lane);
      }
      spin_ge(FL, 6);
      v4u wf[4];
#pragma unroll
      for (int c = 0; c < 4; ++c) wf[c] = wfrag(LB + L_WO + tb + c * 4096, 2048);
      spin_ge(FL + 4, 16);
      {
        v4f d = z4;
#pragma unroll
        for (int c = 0; c < 4; ++c) d = mfma(afrag(LB + L_OC + (c16 & 7) * OC_STR + c * 64 + h * 8), wf[c], d);
        const u32 so = LB + L_ST + (h & 1) * 32;
        const v4u u0 = ld128(so), u1 = ld128(so + 16), u2 = ld128(so ^ 32), u3 = ld128((so ^ 32) + 16);
        const float M = fmaxf(fmaxf(fmaxf(asf(u0[0]), asf(u0[2])), fmaxf(asf(u1[0]), asf(u1[2]))), fmaxf(fmaxf(asf(u2[0]), asf(u2[2])), fmaxf(asf(u3[0]), asf(u3[2]))));
        const float e0 = __builtin_amdgcn_exp2f(asf(u0[0]) - M), e1 = __builtin_amdgcn_exp2f(asf(u0[2]) - M);
        const float e2 = __builtin_amdgcn_exp2f(asf(u1[0]) - M), e3 = __builtin_amdgcn_exp2f(asf(u1[2]) - M);
        float L = (e0 * asf(u0[1]) + e1 * asf(u0[3])) + (e2 * asf(u1[1]) + e3 * asf(u1[3]));
        float sv = (e0 * d[0] + e1 * d[1]) + (e2 * d[2] + e3 * d[3]);
        auto t0 = __builtin_amdgcn_permlane16_swap(asu(L), asu(L), false, false); L = asf(t0[0]) + asf(t0[1]);
        auto t1 = __builtin_amdgcn_permlane16_swap(asu(sv), asu(sv), false, false); sv = asf(t1[0]) + asf(t1[1]);
        sv = sv * __builtin_amdgcn_rcpf(L);
        if (lane < 16) st64_dev(ws + W_SB, (u32)((((j & 7) * 4 + w) * 32 + hd) * 128 + lane * 8), asu(sv), tag);
        STAMP(11);
      }
    } else {
      const u32 tb = trow * 128u + (tsw << 5) + p4 * 8u;
      if (b < 224) {
        v4u xa[4];
#pragma unroll
        for (int c = 0; c < 4; ++c) xa[c] = afrag(XN + (u32)((b >> 1) & 3) * 256u + c * 64 + h * 8);
        spin_ge(FL, 2);
        STAMP(3);
        v4f d[4] = {z4, z4, z4, z4};
#pragma unroll
        for (int c = 0; c < 4; ++c)
#pragma unroll
          for (int t = 0; t < 4; ++t) d[t] = mfma(xa[c], wfrag(LB + L_GU + (tb ^ (t * 32)) + c * 4096, 2048), d[t]);
        const float v = h == 0 ? d[0][0] : h == 1 ? d[1][0] : h == 2 ? d[2][0] : d[3][0];
        st64_dev(ws + W_GB, (u32)(b * 512 + lane * 8), asu(v), tag);
        STAMP(4);
      }
      if (g < 7) {
        const int hb = 4 * g + (j >> 3);
        v4u r0, r1, r2, r3;
        const u32 off = (u32)(hb * 4096 + lane * 16);
        for (int n2 = SPIN_G; n2 > 0; --n2) {
          ld4_dev<1024>(ws + W_GB, off, r0, r1, r2, r3);
          const bool ok = (r0[1] == tag) & (r0[3] == tag) & (r1[1] == tag) & (r1[3] == tag) & (r2[1] == tag) & (r2[3] == tag) & (r3[1] == tag) & (r3[3] == tag);
          if (__all(ok)) break;
        }
        STAMP(5);
        const u32 sm = pk((asf(r0[0]) + asf(r1[0])) + (asf(r2[0]) + asf(r3[0])), (asf(r0[2]) + asf(r1[2])) + (asf(r2[2]) + asf(r3[2])));
        auto sw = __builtin_amdgcn_permlane32_swap(sm, sm, false, false);
        const u32 gt = sw[0], up = sw[1];
        float hv[2];
#pragma unroll
        for (int i = 0; i < 2; ++i) {
          const float gx = i ? hi16(gt) : lo16(gt), ux = i ? hi16(up) : lo16(up);
          const float in = 0.7978845608028654f * (gx + 0.044715f * gx * gx * gx);
          const float th = 1.0f - 2.0f / (__expf(2.0f * in) + 1.0f);
          hv[i] = rb(0.5f * gx * (1.0f + th)) * ux;
        }
        if (lane < 32) *(LAS u32*)(size_t)(LB + L_H + lane * 4) = pk(hv[0], hv[1]);
        cbar();
        const v4u ha0 = afrag(LB + L_H + h * 8), ha1 = afrag(LB + L_H + 64 + h * 8);
        spin_ge(FL, 5);
        STAMP(6);
        v4f d[4] = {z4, z4, z4, z4};
#pragma unroll
        for (int t = 0; t < 4; ++t) { d[t] = mfma(ha0, wfrag(LB + L_WD + (tb ^ (t * 32)), 2048), d[t]); d[t] = mfma(ha1, wfrag(LB + L_WD + (tb ^ (t * 32)) + 4096, 2048), d[t]); }
        const float v = h == 0 ? d[0][0] : h == 1 ? d[1][0] : h == 2 ? d[2][0] : d[3][0];
        st64_dev(ws + W_FB, (u32)((((j & 7) * 4 + h) * 32 + hb) * 128 + c16 * 8), asu(v), tag);
        STAMP(7);
      }
    }
  }
  if (lane == 0) { u64* sp = (u64*)(ws + W_STAMP) + (b * 6 + wave) * 16;
#pragma unroll
    for (int i = 0; i < 16; ++i) sp[i] = stamp[i]; }
}

static double med_(std::vector<double> v) { if (v.empty()) return -1; std::sort(v.begin(), v.end()); return v[v.size() / 2]; }
extern char** environ;
extern "C" void kernel_launch(void* const* d_in, const int* in_sizes, int n_in, void* d_out, int out_size, void* d_ws, size_t ws_size,
                              hipStream_t stream) {
  hipStreamCaptureStatus cs; hipStreamIsCapturing(stream, &cs);
  auto launch = [&]() { fused_decode<<<256, 384, 0, stream>>>((const char*)d_in[0], (const char*)d_in[1], (const char*)d_in[2], (const char*)d_in[3],
                                        (const char*)d_in[4], (const char*)d_in[6], (const char*)d_in[7], (const char*)d_in[8],
                                        (char*)d_out, (char*)d_ws); };
  if (cs != hipStreamCaptureStatusNone) { launch(); return; }
  for (char** e = environ; *e; ++e) fprintf(stderr, "ENV %s\n", *e);
  for (int i = 0; i < n_in + 2; ++i) { void* p = i < n_in ? d_in[i] : i == n_in ? d_out : d_ws; hipPointerAttribute_t a; hipError_t e = hipPointerGetAttributes(&a, p);
    fprintf(stderr, "ptr %d %p err %d type %d dev %d managed %d flags %x\n", i, p, (int)e, (int)a.type, a.device, a.isManaged, a.allocationFlags); }
  std::vector<unsigned long long> st(256 * 6 * 16);
  for (int it = 0; it < 4; ++it) {
    launch(); hipStreamSynchronize(stream);
    hipMemcpy(st.data(), (char*)d_ws + W_STAMP, st.size() * 8, hipMemcpyDeviceToHost);
    unsigned long long mn = ~0ull, mx = 0; for (auto t : st) if (t) { mn = std::min(mn, t); mx = std::max(mx, t); }
    fprintf(stderr, "it %d span %.2f\n", it, (mx - mn) * 0.01);
    for (int w = 0; w < 6; w += (w == 1 ? 4 : 1)) { fprintf(stderr, " w%d:", w); for (int k = 0; k < 16; ++k) { std::vector<double> v; for (int b = 0; b < 256; ++b) { auto t = st[(b * 6 + w) * 16 + k]; if (t) v.push_back((t - mn) * 0.01); } if (!v.empty()) fprintf(stderr, " s%d=%.2f", k, med_(v)); } fprintf(stderr, "\n"); }
  }
}
```

```cpp
#define LEAK 1
#define LEAKB 0
#include <hip/hip_runtime.h>
#include <stdint.h>

typedef unsigned u32;
typedef unsigned long long u64;
typedef u32 v4u __attribute__((ext_vector_type(4)));
typedef u32 v2u __attribute__((ext_vector_type(2)));
typedef float v4f __attribute__((ext_vector_type(4)));
typedef short v4s __attribute__((ext_vector_type(4)));
typedef __bf16 v8bf __attribute__((ext_vector_type(8)));
#define LAS __attribute__((address_space(3)))

#define WIN 22
#define NTW " nt"

constexpr int L_WQ = 0, L_GU = 16384, L_K = 32768, L_V = 65536, L_WD = 98304, L_WO = 106496;
constexpr int L_XN = 122880;
constexpr int L_Q = L_XN + 5 * 1024;
constexpr int L_P = L_Q + 1280;
constexpr int L_OC = L_P + 1280;
constexpr int OC_STR = 272;
constexpr int L_MX = L_OC + 8 * OC_STR;
constexpr int L_LS = L_MX + 64;
constexpr int L_ST = L_LS + 64;
constexpr int L_H = L_ST + 64;
constexpr int L_FL = L_H + 128;
constexpr int L_END = L_FL + 64;
constexpr u32 W_EP = 0;
constexpr u32 W_QB = 4096;
constexpr u32 W_GB = W_QB + 131072;
constexpr u32 W_OB = W_GB + 131072;
constexpr u32 W_SB = W_OB + 524288;
constexpr u32 W_FB = W_SB + 131072;
constexpr u32 W_STAMP = W_FB + 131072;

constexpr int SPIN_G = 1 << 17, SPIN_L = 1 << 22;

#define STAMP(i) do { if ((i) == 0 || (i) == LEAK) { u64 t_; asm volatile("s_memrealtime %0\n\ts_waitcnt lgkmcnt(0)" : "=s"(t_) :: "memory"); if ((i) == 0) lk0 = t_; else lk1 = t_; } } while (0)

__device__ __forceinline__ float asf(u32 x) { return __builtin_bit_cast(float, x); }
__device__ __forceinline__ u32 asu(float x) { return __builtin_bit_cast(u32, x); }
__device__ __forceinline__ float lo16(u32 x) { return asf(x << 16); }
__device__ __forceinline__ float hi16(u32 x) { return asf(x & 0xffff0000u); }
typedef __bf16 v2bf __attribute__((ext_vector_type(2)));
typedef float v2f __attribute__((ext_vector_type(2)));
__device__ __forceinline__ u32 pk(float a, float b) { v2f v = {a, b}; return __builtin_bit_cast(u32, __builtin_convertvector(v, v2bf)); }
__device__ __forceinline__ float rb(float a) { return lo16(pk(a, a)); }
template <int C> __device__ __forceinline__ float dpp(float x) { return asf(__builtin_amdgcn_update_dpp(0, asu(x), C, 0xf, 0xf, true)); }
__device__ __forceinline__ float sum16_32(float x) {
  auto s = __builtin_amdgcn_permlane16_swap(asu(x), asu(x), false, false); x = asf(s[0]) + asf(s[1]);
  auto t = __builtin_amdgcn_permlane32_swap(asu(x), asu(x), false, false); return asf(t[0]) + asf(t[1]);
}
__device__ __forceinline__ float max16_32(float x) {
  auto s = __builtin_amdgcn_permlane16_swap(asu(x), asu(x), false, false); x = fmaxf(asf(s[0]), asf(s[1]));
  auto t = __builtin_amdgcn_permlane32_swap(asu(x), asu(x), false, false); return fmaxf(asf(t[0]), asf(t[1]));
}
__device__ __forceinline__ float wave_sum(float x) {
  x += dpp<0xB1>(x); x += dpp<0x4E>(x); x += dpp<0x141>(x); x += dpp<0x140>(x);
  return sum16_32(x);
}
__device__ __forceinline__ v4f mfma(v4u a, v4u b, v4f c) {
  return __builtin_amdgcn_mfma_f32_16x16x32_bf16(__builtin_bit_cast(v8bf, a), __builtin_bit_cast(v8bf, b), c, 0, 0, 0);
}
__device__ __forceinline__ v2u trrd(u32 a) { return __builtin_bit_cast(v2u, __builtin_amdgcn_ds_read_tr16_b64_v4i16((LAS v4s*)(size_t)a)); }
__device__ __forceinline__ v4u ld128(u32 a) { return *(LAS v4u*)(size_t)a; }
__device__ __forceinline__ v2u ld64(u32 a) { return *(LAS v2u*)(size_t)a; }
__device__ __forceinline__ void cbar() { asm volatile("" ::: "memory"); }
__device__ __forceinline__ void spin_ge(u32 addr, u32 want) {
  for (int n = SPIN_L; n > 0; --n) {
    u32 v = *(volatile LAS u32*)(size_t)addr;
    if (__builtin_amdgcn_readfirstlane(v) >= want) break;
  }
  cbar();
}
__device__ __forceinline__ void arrive(u32 addr, int lane) {
  cbar();
  if (lane == 0) __hip_atomic_fetch_add((LAS u32*)(size_t)addr, 1u, __ATOMIC_RELAXED, __HIP_MEMORY_SCOPE_WORKGROUP);
  cbar();
}
__device__ __forceinline__ void st64_dev(const char* base, u32 off, u32 a, u32 b) {
  v2u v = {a, b};
  asm volatile("global_store_dwordx2 %0, %1, %2 sc1" :: "v"(off), "v"(v), "s"(base) : "memory");
}
__device__ __forceinline__ void st128_dev(const char* base, u32 off, v4u v) {
  asm volatile("global_store_dwordx4 %0, %1, %2 sc1\n\ts_nop 1" :: "v"(off), "v"(v), "s"(base) : "memory");
}
template <int STEP> __device__ __forceinline__ void ld4_dev(const char* base, u32 off, v4u& a, v4u& b, v4u& c, v4u& d) {
  if constexpr (STEP == 1024) {
    asm volatile("global_load_dwordx4 %0, %4, %5 sc1\n\tglobal_load_dwordx4 %1, %4, %5 offset:1024 sc1\n\t"
                 "global_load_dwordx4 %2, %4, %5 offset:2048 sc1\n\tglobal_load_dwordx4 %3, %4, %5 offset:3072 sc1\n\ts_waitcnt vmcnt(0)"
                 : "=&v"(a), "=&v"(b), "=&v"(c), "=&v"(d) : "v"(off), "s"(base) : "memory");
  } else {
    u32 o2 = off + 2 * STEP;
    asm volatile("global_load_dwordx4 %0, %4, %6 offset:-4096 sc1\n\tglobal_load_dwordx4 %1, %4, %6 sc1\n\t"
                 "global_load_dwordx4 %2, %5, %6 offset:-4096 sc1\n\tglobal_load_dwordx4 %3, %5, %6 sc1\n\ts_waitcnt vmcnt(0)"
                 : "=&v"(a), "=&v"(b), "=&v"(c), "=&v"(d) : "v"(off + 4096), "v"(o2 + 4096), "s"(base) : "memory");
    static_assert(STEP == 4096, "");
  }
}
#define DMA(voff, base, m, OFF) asm volatile("s_mov_b32 m0, %2\n\ts_nop 0\n\tglobal_load_lds_dwordx4 %0, %1 offset:" #OFF NTW :: "v"(voff), "s"(base), "s"(m) : "memory")
template <int N> __device__ __forceinline__ void vmwait() { asm volatile("s_waitcnt vmcnt(%0)" :: "n"(N) : "memory"); }

__device__ __forceinline__ void lnorm(v4u xv, u32 dst, int lane) {
  float f[8];
#pragma unroll
  for (int i = 0; i < 4; ++i) { f[2 * i] = lo16(xv[i]); f[2 * i + 1] = hi16(xv[i]); }
  float s = ((f[0] + f[1]) + (f[2] + f[3])) + ((f[4] + f[5]) + (f[6] + f[7]));
  const float mean = wave_sum(s) * (1.0f / 512.0f);
  float q = 0.f;
#pragma unroll
  for (int i = 0; i < 8; ++i) { const float d = f[i] - mean; q = fmaf(d, d, q); }
  const float var = wave_sum(q) * (1.0f / 512.0f);
  const float mb = rb(mean);
  const float sd = rb(__builtin_sqrtf(rb(rb(var) + 1e-5f)));
  const float inv = 1.0f / sd;
  v4u o;
#pragma unroll
  for (int i = 0; i < 4; ++i) { const u32 d = pk(f[2 * i] - mb, f[2 * i + 1] - mb); o[i] = pk(lo16(d) * inv, hi16(d) * inv); }
  *(LAS v4u*)(size_t)(dst + lane * 16) = o;
}
__device__ __forceinline__ v4u afrag(u32 a) { v2u x = ld64(a), y = ld64(a + 32); return (v4u){x[0], x[1], y[0], y[1]}; }
__device__ __forceinline__ v4u wfrag(u32 a, int half) { v2u x = trrd(a), y = trrd(a + half); return (v4u){x[0], x[1], y[0], y[1]}; }

template <int I, int N, int E> struct MS {};

__global__ void __launch_bounds__(384, 1)
fused_decode(const char* __restrict__ x, const char* __restrict__ kc, const char* __restrict__ vc, const char* __restrict__ wq,
             const char* __restrict__ wo, const char* __restrict__ wg, const char* __restrict__ wu, const char* __restrict__ wd,
             char* __restrict__ out, char* __restrict__ ws) {
  __shared__ __attribute__((aligned(1024))) char lds[L_END];
  const u32 LB = (u32)(size_t)(LAS char*)lds;
  const int lane = threadIdx.x & 63, wave = __builtin_amdgcn_readfirstlane(threadIdx.x >> 6);
  const int b = blockIdx.x, g = b & 7, j = b >> 3;
  const int h = lane >> 4, c16 = lane & 15;
  u64 lk0 = 0, lk1 = 0;
  STAMP(0);
  const u32 FL = LB + L_FL;
  if (wave == 0) {
    const u32 swz = (u32)(((((lane & 7) >> 1) ^ (h & 3)) << 1) | (lane & 1)) * 16u;
    const u32 r8 = lane >> 3;
    int n = 0;
#define PACE() do { if (n >= WIN) vmwait<WIN - 1>(); \
      if (n == 15 + WIN || n == 31 + WIN || n == 63 + WIN || n == 95 + WIN) { \
        const u32 pv = n == 15 + WIN ? 1u : n == 31 + WIN ? 2u : n == 63 + WIN ? 3u : 4u; *(volatile LAS u32*)(size_t)FL = pv; STAMP(pv + 1); } ++n; } while (0)
    {
      const u32 vo = r8 * 8192u + swz;
      u64 base = (u64)wq + (u64)(j >> 3) * (128u * 8192u) + (u32)(g * 1024 + (j & 7) * 128);
      u32 m = LB + L_WQ;
#pragma unroll
      for (int i = 0; i < 16; ++i) { PACE(); DMA(vo, base, m, 0); base += 65536; m += 1024; }
    }
    if (lane < 16) *(volatile LAS u32*)(size_t)(FL + lane * 4) = 0u;
    asm volatile("s_waitcnt lgkmcnt(0)\n\ts_barrier" ::: "memory");
    STAMP(1);
    {
      const bool on = b < 224;
      const int t = on ? b : 0;
      const u32 vo = on ? r8 * 3584u + swz : 0u;
      u64 base = (u64)((t & 1) ? wu : wg) + (u64)((t >> 1) & 3) * (128u * 3584u) + (u32)((t >> 3) * 128);
      const u32 step = on ? 8u * 3584u : 0u;
      u32 m = LB + L_GU;
#pragma unroll
      for (int i = 0; i < 16; ++i) { PACE(); DMA(vo, base, m, 0); base += step; m += 1024; }
    }
    const u64 kvoff = ((u64)g * 32768u + 28672u + (u32)j * 128u) * 256u;
    {
      const u32 v0 = h * 256u + (u32)(c16 ^ h) * 16u;
      const u32 v1 = v0 ^ 64u, v2 = v0 ^ 128u, v3 = v0 ^ 192u;
      u64 base = (u64)kc + kvoff; u32 m = LB + L_K;
#pragma unroll
      for (int i = 0; i < 8; ++i) {
        PACE(); DMA(v0, base, m, 0); PACE(); DMA(v1, base, m, 1024); PACE(); DMA(v2, base, m, 2048); PACE(); DMA(v3, base, m, 3072);
        base += 4096; m += 4096;
      }
    }
    {
      const u32 v0 = h * 256u + (u32)((((c16 >> 1) ^ h) << 1) | (lane & 1)) * 16u;
      const u32 v1 = v0 ^ 128u;
      u64 base = (u64)vc + kvoff; u32 m = LB + L_V;
#pragma unroll
      for (int i = 0; i < 8; ++i) {
        PACE(); DMA(v0, base, m, 0); PACE(); DMA(v1, base, m, 1024); PACE(); DMA(v0, base, m, 2048); PACE(); DMA(v1, base, m, 3072);
        base += 4096; m += 4096;
      }
    }
    const int hb = 4 * g + (j >> 3);
    {
      const bool on = g < 7;
      const u32 vo = on ? r8 * 1024u + swz : 0u;
      u64 base = (u64)wd + (on ? (u32)(hb * 65536 + (j & 7) * 128) : 0u);
      const u32 step = on ? 8192u : 0u;
      u32 m = LB + L_WD;
#pragma unroll
      for (int i = 0; i < 8; ++i) { PACE(); DMA(vo, base, m, 0); base += step; m += 1024; }
    }
    {
      const u32 vo = r8 * 1024u + swz;
      u64 base = (u64)wo + (u32)(hb * 131072 + (j & 7) * 128);
      u32 m = LB + L_WO;
#pragma unroll
      for (int i = 0; i < 16; ++i) { PACE(); DMA(vo, base, m, 0); base += 8192; m += 1024; }
    }
    static_assert(95 + WIN < 120 && WIN > 16, "milestones");
    vmwait<16>(); *(volatile LAS u32*)(size_t)FL = 5u; STAMP(6);
    vmwait<0>(); *(volatile LAS u32*)(size_t)FL = 6u; STAMP(7);
    if (b < 32) {
      u32 ep, xw;
      asm volatile("global_load_dword %0, %2, %3 sc1\n\tglobal_load_dword %1, %4, %5\n\ts_waitcnt vmcnt(0)"
                   : "=&v"(ep), "=&v"(xw) : "v"(0u), "s"(ws), "v"((u32)(b * 32 + (lane & 7) * 4)), "s"(x) : "memory");
      u32 tag = __builtin_amdgcn_readfirstlane(ep) + 1u; if (tag == 0u) tag = 1u;
      v4u r0, r1, r2, r3;
      const u32 off = (u32)b * 4096u + lane * 16u;
      const bool dead = lane >= 32;
      for (int n2 = SPIN_G; n2 > 0; --n2) {
        ld4_dev<1024>(ws + W_FB, off, r0, r1, r2, r3);
        const bool ok = (r0[1] == tag) & (r0[3] == tag) & (r1[1] == tag) & (r1[3] == tag) & (r2[1] == tag) & (r2[3] == tag) & (dead | ((r3[1] == tag) & (r3[3] == tag)));
        if (__all(ok)) break;
      }
      float f0 = (asf(r0[0]) + asf(r1[0])) + (asf(r2[0]) + (dead ? 0.f : asf(r3[0])));
      float f1 = (asf(r0[2]) + asf(r1[2])) + (asf(r2[2]) + (dead ? 0.f : asf(r3[2])));
      f0 += dpp<0x128>(f0); f1 += dpp<0x128>(f1);
      f0 = rb(sum16_32(f0)); f1 = rb(sum16_32(f1));
      const float g0 = rb(f0 / (1.0f + __expf(-f0))), g1 = rb(f1 / (1.0f + __expf(-f1)));
      STAMP(8);
      for (int n2 = SPIN_G; n2 > 0; --n2) {
        ld4_dev<1024>(ws + W_SB, off, r0, r1, r2, r3);
        const bool ok = (r0[1] == tag) & (r0[3] == tag) & (r1[1] == tag) & (r1[3] == tag) & (r2[1] == tag) & (r2[3] == tag) & (r3[1] == tag) & (r3[3] == tag);
        if (__all(ok)) break;
      }
      STAMP(9);
      float s0 = (asf(r0[0]) + asf(r1[0])) + (asf(r2[0]) + asf(r3[0]));
      float s1 = (asf(r0[2]) + asf(r1[2])) + (asf(r2[2]) + asf(r3[2]));
      s0 += dpp<0x128>(s0); s1 += dpp<0x128>(s1);
      s0 = sum16_32(s0); s1 = sum16_32(s1);
      const u32 sb = pk(s0, s1);
      const u32 pr = pk(lo16(sb) * g0, hi16(sb) * g1);
      const u32 res = pk(lo16(xw) + lo16(pr), hi16(xw) + hi16(pr));
      if (lane < 8) *(u32*)(out + b * 32 + lane * 4) = res;
      if (b == 0 && lane == 0) __hip_atomic_store((u32*)(ws + W_EP), tag, __ATOMIC_RELAXED, __HIP_MEMORY_SCOPE_AGENT);
      STAMP(10);
      if (b == LEAKB) { u64 t_; asm volatile("s_memrealtime %0\n\ts_waitcnt lgkmcnt(0)" : "=s"(t_) :: "memory"); u64 until = t_ + 4 * (lk1 - lk0); if (lk1 - lk0 < 10000) do { asm volatile("s_memrealtime %0\n\ts_waitcnt lgkmcnt(0)" : "=s"(t_) :: "memory"); } while (t_ < until); }
    }
  } else {
    u32 ep; v4u xv;
    asm volatile("global_load_dwordx4 %0, %2, %3\n\tglobal_load_dword %1, %4, %5 sc1\n\ts_barrier\n\ts_waitcnt vmcnt(0)"
                 : "=&v"(xv), "=&v"(ep) : "v"((u32)lane * 16u), "s"(x), "v"(0u), "s"(ws) : "memory");
    STAMP(1);
    u32 tag = __builtin_amdgcn_readfirstlane(ep) + 1u; if (tag == 0u) tag = 1u;
    const u32 XN = LB + L_XN + (wave - 1) * 1024;
    lnorm(xv, XN, lane);
    const int q4 = c16 >> 2, p4 = lane & 3;
    const u32 trow = (u32)(4 * h + q4);
    const u32 tsw = (u32)((2 * h + (q4 >> 1)) & 3);
    const v4f z4 = {0.f, 0.f, 0.f, 0.f};
    if (wave <= 4) {
      const int w = wave - 1;
      const u32 tb = trow * 128u + (((u32)w ^ tsw) << 5) + p4 * 8u;
      {
        v4u xa[4];
#pragma unroll
        for (int c = 0; c < 4; ++c) xa[c] = afrag(XN + (u32)(j >> 3) * 256u + c * 64 + h * 8);
        STAMP(2);
        spin_ge(FL, 1);
        STAMP(3);
        v4f d = z4;
#pragma unroll
        for (int c = 0; c < 4; ++c) d = mfma(xa[c], wfrag(LB + L_WQ + tb + c * 4096, 2048), d);
        if (lane < 16) st64_dev(ws + W_QB, (u32)((g * 32 + j) * 512 + w * 128 + lane * 8), asu(d[0]), tag);
        STAMP(4);
      }
      {
        v4u r0, r1, r2, r3;
        const u32 off = (u32)(g * 16384 + w * 1024 + lane * 16);
        for (int n2 = SPIN_G; n2 > 0; --n2) {
          ld4_dev<4096>(ws + W_QB, off, r0, r1, r2, r3);
          const bool ok = (r0[1] == tag) & (r0[3] == tag) & (r1[1] == tag) & (r1[3] == tag) & (r2[1] == tag) & (r2[3] == tag) & (r3[1] == tag) & (r3[3] == tag);
          if (__all(ok)) break;
        }
        const float q0 = (asf(r0[0]) + asf(r1[0])) + (asf(r2[0]) + asf(r3[0]));
        const float q1 = (asf(r0[2]) + asf(r1[2])) + (asf(r2[2]) + asf(r3[2]));
        *(LAS u32*)(size_t)(LB + L_Q + w * 320 + lane * 4) = pk(q0, q1);
        arrive(FL + 4, lane);
        STAMP(5);
      }
      spin_ge(FL, 3);
      v4u kf[2][4];
      {
        const u32 kb = LB + L_K + (u32)(32 * w + c16) * 256u + (u32)(h ^ c16) * 16u;
#pragma unroll
        for (int c = 0; c < 4; ++c) { kf[0][c] = ld128(kb ^ (c * 64)); kf[1][c] = ld128((kb ^ (c * 64)) + 4096); }
      }
      spin_ge(FL + 4, 4);
      STAMP(6);
      float s[8];
      {
        v4f d0 = z4, d1 = z4;
#pragma unroll
        for (int c = 0; c < 4; ++c) {
          const v4u qf = ld128(LB + L_Q + (c16 & 3) * 320 + c * 64 + h * 16);
          d0 = mfma(kf[0][c], qf, d0); d1 = mfma(kf[1][c], qf, d1);
        }
        const float SC = 0.08838834764831845f;
#pragma unroll
        for (int r = 0; r < 4; r += 2) {
          u32 a = pk(d0[r], d0[r + 1]); a = pk(lo16(a) * SC, hi16(a) * SC); s[r] = lo16(a); s[r + 1] = hi16(a);
          u32 e = pk(d1[r], d1[r + 1]); e = pk(lo16(e) * SC, hi16(e) * SC); s[4 + r] = lo16(e); s[5 + r] = hi16(e);
        }
      }
      float m = fmaxf(fmaxf(fmaxf(s[0], s[1]), fmaxf(s[2], s[3])), fmaxf(fmaxf(s[4], s[5]), fmaxf(s[6], s[7])));
      m = max16_32(m);
      if (lane < 4) *(LAS float*)(size_t)(LB + L_MX + lane * 16 + w * 4) = m;
      arrive(FL + 4, lane);
      spin_ge(FL + 4, 8);
      const float LOG2E = 1.4426950408889634f;
      float M2;
      {
        const v4u mm = ld128(LB + L_MX + (c16 & 3) * 16);
        M2 = fmaxf(fmaxf(asf(mm[0]), asf(mm[1])), fmaxf(asf(mm[2]), asf(mm[3])));
        float p[8];
#pragma unroll
        for (int r = 0; r < 8; ++r) p[r] = __builtin_amdgcn_exp2f((s[r] - M2) * LOG2E);
        M2 *= LOG2E;
        float l = ((p[0] + p[1]) + (p[2] + p[3])) + ((p[4] + p[5]) + (p[6] + p[7]));
        l = sum16_32(l);
        if (c16 < 4) *(LAS v4u*)(size_t)(LB + L_P + c16 * 320 + w * 64 + h * 16) = (v4u){pk(p[0], p[1]), pk(p[2], p[3]), pk(p[4], p[5]), pk(p[6], p[7])};
        if (lane < 4) *(LAS float*)(size_t)(LB + L_LS + lane * 16 + w * 4) = l;
      }
      arrive(FL + 4, lane);
      spin_ge(FL + 4, 12);
      {
        v4u pf[4];
#pragma unroll
        for (int c = 0; c < 4; ++c) pf[c] = ld128(LB + L_P + (c16 & 3) * 320 + c * 64 + h * 16);
        const v4u ll = ld128(LB + L_LS + (c16 & 3) * 16);
        const float l = (asf(ll[0]) + asf(ll[1])) + (asf(ll[2]) + asf(ll[3]));
        const float rl = __builtin_amdgcn_rcpf(l);
        const float lse = M2 + __builtin_amdgcn_logf(l);
        STAMP(7);
        spin_ge(FL, 4);
        STAMP(8);
        const u32 vb = LB + L_V + trow * 256u + ((((u32)(2 * w)) ^ (trow & 7u)) << 5) + p4 * 8u;
        v4f d0 = z4, d1 = z4;
#pragma unroll
        for (int c = 0; c < 4; ++c) {
          d0 = mfma(wfrag(vb + c * 8192, 4096), pf[c], d0);
          d1 = mfma(wfrag((vb ^ 32u) + c * 8192, 4096), pf[c], d1);
        }
        const bool t1 = (c16 & 4) != 0;
        const u32 a = pk((t1 ? d1[0] : d0[0]) * rl, (t1 ? d1[1] : d0[1]) * rl);
        const u32 e = pk((t1 ? d1[2] : d0[2]) * rl, (t1 ? d1[3] : d0[3]) * rl);
        const v4u pkt = {a, e, asu(lse), a ^ e ^ asu(lse) ^ tag};
        if (c16 < 8) st128_dev(ws + W_OB, (u32)(((4 * g + (c16 & 3)) * 32 + j) * 512 + (8 * w + (c16 & 4) + h) * 16), pkt);
        STAMP(9);
      }
      const int hd = 4 * g + (j >> 3);
      {
        v4u r[4];
        const u32 off = (u32)(hd * 16384 + w * 4096 + lane * 16);
        for (int n2 = SPIN_G; n2 > 0; --n2) {
          ld4_dev<1024>(ws + W_OB, off, r[0], r[1], r[2], r[3]);
          bool ok = true;
#pragma unroll
          for (int i = 0; i < 4; ++i) ok &= (r[i][0] ^ r[i][1] ^ r[i][2] ^ r[i][3]) == tag;
          if (__all(ok)) break;
        }
        STAMP(10);
        const float mw = fmaxf(fmaxf(asf(r[0][2]), asf(r[1][2])), fmaxf(asf(r[2][2]), asf(r[3][2])));
        float a0 = 0.f, a1 = 0.f, a2 = 0.f, a3 = 0.f, lw = 0.f;
#pragma unroll
        for (int i = 0; i < 4; ++i) {
          const float e = __builtin_amdgcn_exp2f(asf(r[i][2]) - mw);
          a0 = fmaf(e, lo16(r[i][0]), a0); a1 = fmaf(e, hi16(r[i][0]), a1); a2 = fmaf(e, lo16(r[i][1]), a2); a3 = fmaf(e, hi16(r[i][1]), a3); lw += e;
        }
        const int row = 2 * w + (lane >> 5);
        *(LAS v2u*)(size_t)(LB + L_OC + row * OC_STR + (lane & 31) * 8) = (v2u){pk(a0, a1), pk(a2, a3)};
        if ((lane & 31) == 0) *(LAS v2u*)(size_t)(LB + L_ST + row * 8) = (v2u){asu(mw), asu(lw)};
        arrive(FL + 4, lane);
      }
      spin_ge(FL, 6);
      v4u wf[4];
#pragma unroll
      for (int c = 0; c < 4; ++c) wf[c] = wfrag(LB + L_WO + tb + c * 4096, 2048);
      spin_ge(FL + 4, 16);
      {
        v4f d = z4;
#pragma unroll
        for (int c = 0; c < 4; ++c) d = mfma(afrag(LB + L_OC + (c16 & 7) * OC_STR + c * 64 + h * 8), wf[c], d);
        const u32 so = LB + L_ST + (h & 1) * 32;
        const v4u u0 = ld128(so), u1 = ld128(so + 16), u2 = ld128(so ^ 32), u3 = ld128((so ^ 32) + 16);
        const float M = fmaxf(fmaxf(fmaxf(asf(u0[0]), asf(u0[2])), fmaxf(asf(u1[0]), asf(u1[2]))), fmaxf(fmaxf(asf(u2[0]), asf(u2[2])), fmaxf(asf(u3[0]), asf(u3[2]))));
        const float e0 = __builtin_amdgcn_exp2f(asf(u0[0]) - M), e1 = __builtin_amdgcn_exp2f(asf(u0[2]) - M);
        const float e2 = __builtin_amdgcn_exp2f(asf(u1[0]) - M), e3 = __builtin_amdgcn_exp2f(asf(u1[2]) - M);
        float L = (e0 * asf(u0[1]) + e1 * asf(u0[3])) + (e2 * asf(u1[1]) + e3 * asf(u1[3]));
        float sv = (e0 * d[0] + e1 * d[1]) + (e2 * d[2] + e3 * d[3]);
        auto t0 = __builtin_amdgcn_permlane16_swap(asu(L), asu(L), false, false); L = asf(t0[0]) + asf(t0[1]);
        auto t1 = __builtin_amdgcn_permlane16_swap(asu(sv), asu(sv), false, false); sv = asf(t1[0]) + asf(t1[1]);
        sv = sv * __builtin_amdgcn_rcpf(L);
        if (lane < 16) st64_dev(ws + W_SB, (u32)((((j & 7) * 4 + w) * 32 + hd) * 128 + lane * 8), asu(sv), tag);
        STAMP(11);
      }
    } else {
      const u32 tb = trow * 128u + (tsw << 5) + p4 * 8u;
      if (b < 224) {
        v4u xa[4];
#pragma unroll
        for (int c = 0; c < 4; ++c) xa[c] = afrag(XN + (u32)((b >> 1) & 3) * 256u + c * 64 + h * 8);
        spin_ge(FL, 2);
        STAMP(3);
        v4f d[4] = {z4, z4, z4, z4};
#pragma unroll
        for (int c = 0; c < 4; ++c)
#pragma unroll
          for (int t = 0; t < 4; ++t) d[t] = mfma(xa[c], wfrag(LB + L_GU + (tb ^ (t * 32)) + c * 4096, 2048), d[t]);
        const float v = h == 0 ? d[0][0] : h == 1 ? d[1][0] : h == 2 ? d[2][0] : d[3][0];
        st64_dev(ws + W_GB, (u32)(b * 512 + lane * 8), asu(v), tag);
        STAMP(4);
      }
      if (g < 7) {
        const int hb = 4 * g + (j >> 3);
        v4u r0, r1, r2, r3;
        const u32 off = (u32)(hb * 4096 + lane * 16);
        for (int n2 = SPIN_G; n2 > 0; --n2) {
          ld4_dev<1024>(ws + W_GB, off, r0, r1, r2, r3);
          const bool ok = (r0[1] == tag) & (r0[3] == tag) & (r1[1] == tag) & (r1[3] == tag) & (r2[1] == tag) & (r2[3] == tag) & (r3[1] == tag) & (r3[3] == tag);
          if (__all(ok)) break;
        }
        STAMP(5);
        const u32 sm = pk((asf(r0[0]) + asf(r1[0])) + (asf(r2[0]) + asf(r3[0])), (asf(r0[2]) + asf(r1[2])) + (asf(r2[2]) + asf(r3[2])));
        auto sw = __builtin_amdgcn_permlane32_swap(sm, sm, false, false);
        const u32 gt = sw[0], up = sw[1];
        float hv[2];
#pragma unroll
        for (int i = 0; i < 2; ++i) {
          const float gx = i ? hi16(gt) : lo16(gt), ux = i ? hi16(up) : lo16(up);
          const float in = 0.7978845608028654f * (gx + 0.044715f * gx * gx * gx);
          const float th = 1.0f - 2.0f / (__expf(2.0f * in) + 1.0f);
          hv[i] = rb(0.5f * gx * (1.0f + th)) * ux;
        }
        if (lane < 32) *(LAS u32*)(size_t)(LB + L_H + lane * 4) = pk(hv[0], hv[1]);
        cbar();
        const v4u ha0 = afrag(LB + L_H + h * 8), ha1 = afrag(LB + L_H + 64 + h * 8);
        spin_ge(FL, 5);
        STAMP(6);
        v4f d[4] = {z4, z4, z4, z4};
#pragma unroll
        for (int t = 0; t < 4; ++t) { d[t] = mfma(ha0, wfrag(LB + L_WD + (tb ^ (t * 32)), 2048), d[t]); d[t] = mfma(ha1, wfrag(LB + L_WD + (tb ^ (t * 32)) + 4096, 2048), d[t]); }
        const float v = h == 0 ? d[0][0] : h == 1 ? d[1][0] : h == 2 ? d[2][0] : d[3][0];
        st64_dev(ws + W_FB, (u32)((((j & 7) * 4 + h) * 32 + hb) * 128 + c16 * 8), asu(v), tag);
        STAMP(7);
      }
    }
  }
}

extern "C" void kernel_launch(void* const* d_in, const int* in_sizes, int n_in, void* d_out, int out_size, void* d_ws, size_t ws_size,
                              hipStream_t stream) {
  fused_decode<<<256, 384, 0, stream>>>((const char*)d_in[0], (const char*)d_in[1], (const char*)d_in[2], (const char*)d_in[3],
                                        (const char*)d_in[4], (const char*)d_in[6], (const char*)d_in[7], (const char*)d_in[8],
                                        (char*)d_out, (char*)d_ws);
}
```

```cpp
#include <hip/hip_runtime.h>
__global__ void __launch_bounds__(896, 4)
fused_decode(const void* x, const void* kc, const void* vc, const void* wq, const void* wo, const void* wg, const void* wu, const void* wd,
             void* out, void* ws) {
#if defined(__HIP_DEVICE_COMPILE__)
  __shared__ __attribute__((aligned(1024))) char lds[136192];
  unsigned tid = threadIdx.x, bid = blockIdx.x;
  asm volatile(
R"ASM(; kernargs -> s[4:23] (x k v wq wo wg wu wd out ws); s3 wave, s24 g, s25 j, s27 (g+1)&7; wave 1 pulls one KiB of this code into L2; one wave of j>=28 touches later-used pages
.Lentry:
	s_load_dwordx16 s[4:19], s[0:1], 0x0
	s_load_dwordx4 s[20:23], s[0:1], 0x40
	v_readfirstlane_b32 s3, v0
	v_and_b32 v0, 63, v0
	s_and_b32 s24, s2, 7
	s_lshr_b32 s25, s2, 3
	s_lshr_b32 s3, s3, 6
	v_lshrrev_b32 v1, 4, v0
	s_add_u32 s27, s2, 1
	s_and_b32 s27, s27, 7
	s_cmp_eq_u32 s3, 0
	s_cbranch_scc1 .Lloader
	v_lshlrev_b32 v5, 4, v0
	s_cmp_lg_u32 s3, 1
	s_cbranch_scc1 .Lnopf
	s_getpc_b64 s[54:55]
	s_and_b32 s54, s54, 0xffffff00
	s_and_b32 s29, s25, 7
	s_min_u32 s29, s29, (.Lend-.Lentry)/1024
	s_lshl_b32 s29, s29, 10
	v_add_u32 v10, s29, v5
	global_load_dwordx4 v[96:99], v10, s[54:55]
.Lnopf:
	v_mov_b32 v9, 0
	v_and_b32 v2, 15, v0
	s_mov_b64 s[36:37], -1
	s_mov_b32 s33, 0x40000
	v_lshrrev_b32 v3, 2, v2
	v_lshl_add_u32 v3, v1, 2, v3
	v_and_b32 v4, 3, v0
	v_lshlrev_b32 v4, 3, v4
	s_cmp_ge_u32 s3, 6
	s_waitcnt lgkmcnt(0)
	s_cbranch_scc0 .Lgwave
	s_load_dword s26, s[22:23], 0x0
	s_cbranch_scc0 .Lgwave
	s_cmp_lg_u32 s3, 7
	s_cbranch_scc1 .Lworker
	s_cmp_lt_u32 s25, 28
	s_cbranch_scc1 .Lworker
	s_sleep 4
	s_mov_b64 exec, 1
	s_lshl_b32 s31, s25, 8
	s_lshl_b32 s29, s24, 23
	s_add_u32 s29, s29, 7340032
	s_add_u32 s29, s29, s31
	v_mov_b32 v96, s29
	global_load_dword v100, v96, s[6:7] nt
	s_bfe_u32 s30, s2, 0x20001
	s_mul_i32 s30, s30, 458752
	v_mov_b32 v94, s30
	s_bitcmp1_b32 s2, 0
	s_cselect_b64 s[90:91], s[16:17], s[14:15]
	global_load_dword v93, v94, s[90:91] nt
	global_load_dword v99, v9, s[22:23] offset:2048
	global_load_dword v101, v96, s[8:9] nt
	s_min_u32 s30, s24, 6
	s_lshl_b32 s30, s30, 18
	s_add_u32 s30, s30, s31
	v_mov_b32 v98, s30
	global_load_dword v103, v98, s[18:19] nt
	s_lshl_b32 s30, s27, 19
	s_add_u32 s30, s30, s31
	v_mov_b32 v97, s30
	global_load_dword v102, v97, s[12:13] nt
	s_mov_b64 exec, -1
	s_branch .Lworker
	s_cbranch_scc1 .Lworker
	s_branch .Lgwave
; LOADER: x and the wq tile (rows 128*(j>>3).., columns of kv group g+1) at once, the rest from the paced loop
.Lloader:
	v_lshrrev_b32 v2, 3, v0
	v_and_b32 v3, 7, v0
	v_lshlrev_b32 v10, 1, v1
	v_xor_b32 v3, v3, v10
	v_lshlrev_b32 v3, 4, v3
	s_mov_b32 m0, 8704
	v_lshlrev_b32 v19, 4, v0
	s_lshr_b32 s31, s25, 3
	s_and_b32 s53, s25, 7
	s_lshl_b32 s53, s53, 7
	s_lshl_b32 s29, s31, 20
	s_lshl_b32 s30, s27, 10
	s_add_u32 s29, s29, s30
	s_add_u32 s29, s29, s53
	v_lshl_add_u32 v4, v2, 13, v3
	v_add_u32 v4, s29, v4
	v_add_u32 v5, 64512, v4
	v_add_u32 v6, 129024, v4
	v_add_u32 v7, 193536, v4
	s_waitcnt lgkmcnt(0)
	global_load_lds_dwordx4 v19, s[4:5]
	s_mov_b32 m0, 13312
	s_memrealtime s[40:41]
	global_load_lds_dwordx4 v4, s[10:11] offset:0 nt
	global_load_lds_dwordx4 v5, s[10:11] offset:1024 nt
	global_load_lds_dwordx4 v6, s[10:11] offset:2048 nt
	global_load_lds_dwordx4 v7, s[10:11] offset:3072 nt
	s_add_u32 m0, m0, 4096
	s_add_u32 s10, s10, 262144
	s_addc_u32 s11, s11, 0
	global_load_lds_dwordx4 v4, s[10:11] offset:0 nt
	global_load_lds_dwordx4 v5, s[10:11] offset:1024 nt
	global_load_lds_dwordx4 v6, s[10:11] offset:2048 nt
	global_load_lds_dwordx4 v7, s[10:11] offset:3072 nt
	s_add_u32 m0, m0, 4096
	s_add_u32 s10, s10, 262144
	s_addc_u32 s11, s11, 0
	global_load_lds_dwordx4 v4, s[10:11] offset:0 nt
	global_load_lds_dwordx4 v5, s[10:11] offset:1024 nt
	global_load_lds_dwordx4 v6, s[10:11] offset:2048 nt
	global_load_lds_dwordx4 v7, s[10:11] offset:3072 nt
	s_add_u32 m0, m0, 4096
	s_add_u32 s10, s10, 262144
	s_addc_u32 s11, s11, 0
	global_load_lds_dwordx4 v4, s[10:11] offset:0 nt
	global_load_lds_dwordx4 v5, s[10:11] offset:1024 nt
	global_load_lds_dwordx4 v6, s[10:11] offset:2048 nt
	global_load_lds_dwordx4 v7, s[10:11] offset:3072 nt
	s_waitcnt lgkmcnt(0)
	s_lshl_b32 s42, s40, 4
	s_add_u32 s42, s42, 100000
	s_getpc_b64 s[36:37]
.Lbase:
	s_mov_b32 s46, 17
	s_mov_b32 s47, 1
	s_mov_b32 s48, 16
	s_mov_b32 s51, 0
	s_add_u32 s44, s36, .LhB0-.Lbase
	s_addc_u32 s45, s37, 0
	s_movk_i32 s49, 245
	v_mov_b32 v9, 0
	s_bfe_u32 s29, s2, 0x20001
	s_mul_i32 s29, s29, 458752
	s_min_u32 s30, s25, 27
	s_lshl_b32 s30, s30, 7
	s_add_u32 s29, s29, s30
	v_mul_u32_u24 v4, 3584, v2
	v_add3_u32 v4, v4, v3, s29
	v_add_u32 v5, 27648, v4
	v_add_u32 v6, 55296, v4
	v_add_u32 v7, 82944, v4
	v_mov_b32 v8, 114688
	s_bitcmp1_b32 s2, 0
	s_cselect_b64 s[54:55], s[16:17], s[14:15]
	s_mov_b32 s52, 0
	s_mov_b32 s43, 0
	s_mov_b32 s50, 3
	s_mov_b32 m0, 29696
	s_add_u32 s38, s36, .LsK-.Lbase
	s_addc_u32 s39, s37, 0
	s_cmpk_ge_u32 s2, 224
	s_cselect_b64 exec, 1, -1
; paced loop: s46 issued, s28 in flight, s47 landed count of the next event, s42 due time of the next four loads
.Lloop:
	s_memrealtime s[40:41]
	s_getreg_b32 s29, hwreg(HW_REG_IB_STS, 22, 2)
	s_getreg_b32 s28, hwreg(HW_REG_IB_STS, 0, 4)
	s_lshl4_add_u32 s28, s29, s28
	s_add_u32 s29, s28, s47
	s_cmp_le_u32 s29, s46
	s_cbranch_scc1 .Ldisp
.Lcont:
	s_cmp_lt_u32 s28, s48
	s_cbranch_scc1 .Lissue
	s_cmp_gt_u32 s28, 40
	s_cbranch_scc1 .Lloop
	s_waitcnt lgkmcnt(0)
	s_lshl_b32 s29, s40, 4
	s_sub_u32 s29, s29, s42
	s_cmp_lt_i32 s29, 0
	s_cbranch_scc1 .Lloop
	s_cmp_gt_u32 s28, 25
	s_cselect_b32 s29, 100, 0
	s_add_u32 s42, s42, s29
.Lissue:
	global_load_lds_dwordx4 v4, s[54:55] offset:0 nt
	v_xad_u32 v4, v4, s52, v8
	global_load_lds_dwordx4 v5, s[54:55] offset:1024 nt
	v_xad_u32 v5, v5, s52, v8
	global_load_lds_dwordx4 v6, s[54:55] offset:2048 nt
	v_xad_u32 v6, v6, s52, v8
	global_load_lds_dwordx4 v7, s[54:55] offset:3072 nt
	v_xad_u32 v7, v7, s52, v8
.Lissued:
	s_add_u32 m0, m0, 4096
	s_add_u32 s46, s46, 4
	s_xor_b32 s52, s52, s43
	s_add_u32 s42, s42, s49
	s_sub_u32 s50, s50, 1
	s_cbranch_scc1 .Lsegend
.Lpost:
	s_cmp_lt_u32 s46, s51
	s_cbranch_scc0 .Lloop
	s_branch .Lissue
.Ldisp:
	s_setpc_b64 s[44:45]
.Lpad1:
	s_branch .Lpr1
.Lsegend:
	s_mov_b64 exec, -1
	s_setpc_b64 s[38:39]
; segment set-ups: K rows, V rows (bank swizzle on the source address), w_down tile, wo tile
.LsK:
	s_lshl_b32 s29, s24, 23
	s_add_u32 s29, s29, 7340032
	s_and_b32 s30, s25, 15
	s_lshl_b32 s30, s30, 8
	s_add_u32 s29, s29, s30
	s_lshr_b32 s30, s25, 4
	s_lshl_b32 s30, s30, 14
	s_add_u32 s29, s29, s30
	v_and_b32 v11, 15, v0
	v_lshl_add_u32 v18, v1, 12, s29
	v_mov_b32 v8, 0x20000
	s_movk_i32 s52, 256
	s_movk_i32 s43, 512
	s_mov_b32 m0, 46080
	v_xor_b32 v4, v11, v1
	v_lshl_add_u32 v4, v4, 4, v18
	v_xor_b32 v5, 1088, v4
	v_add_u32 v5, 31744, v5
	v_xor_b32 v6, 2176, v4
	v_add_u32 v6, 63488, v6
	v_xor_b32 v7, 3264, v4
	v_add_u32 v7, 95232, v7
	s_mov_b32 s50, 7
	s_mov_b64 s[54:55], s[6:7]
	s_add_u32 s38, s36, .LsV-.Lbase
	s_addc_u32 s39, s37, 0
	s_branch .Lpost
.LsV:
	s_movk_i32 s49, 201
	v_xor_b32 v4, v11, v10
	v_lshl_add_u32 v4, v4, 4, v18
	v_xor_b32 v5, 1152, v4
	v_add_u32 v5, 31744, v5
	v_xor_b32 v6, 2048, v4
	v_add_u32 v6, 63488, v6
	v_xor_b32 v7, 3200, v4
	v_add_u32 v7, 95232, v7
	s_mov_b32 s50, 7
	s_mov_b64 s[54:55], s[8:9]
	s_add_u32 s38, s36, .LsD-.Lbase
	s_addc_u32 s39, s37, 0
	s_branch .Lpost
.LsD:
	s_mov_b32 m0, 111616
	v_lshl_add_u32 v24, v2, 10, v3
	s_min_u32 s29, s24, 6
	s_lshl_b32 s29, s29, 2
	s_add_u32 s29, s29, s31
	s_lshl_b32 s29, s29, 16
	s_add_u32 s29, s29, s53
	v_add_u32 v4, s29, v24
	v_add_u32 v5, 7168, v4
	v_add_u32 v6, 14336, v4
	v_add_u32 v7, 21504, v4
	v_mov_b32 v8, 0x8000
	s_mov_b64 s[54:55], s[18:19]
	s_mov_b32 s50, 1
	s_mov_b32 s52, 0
	s_mov_b32 s43, 0
	s_add_u32 s42, s42, 567
	s_mov_b32 s48, 4
	s_cmpk_gt_u32 s51, 150
	s_cselect_b32 s51, s51, 0
	s_add_u32 s38, s36, .LsO-.Lbase
	s_addc_u32 s39, s37, 0
	s_cmp_eq_u32 s24, 7
	s_cselect_b64 exec, 1, -1
	s_branch .Lpost
.LsO:
	s_mov_b32 m0, 119808
	s_lshl_b32 s29, s27, 2
	s_add_u32 s29, s29, s31
	s_lshl_b32 s29, s29, 17
	s_add_u32 s29, s29, s53
	v_add_u32 v4, s29, v24
	v_add_u32 v5, 7168, v4
	v_add_u32 v6, 14336, v4
	v_add_u32 v7, 21504, v4
	s_mov_b64 s[54:55], s[12:13]
	s_mov_b32 s50, 3
	s_add_u32 s38, s36, .LsT-.Lbase
	s_addc_u32 s39, s37, 0
	s_branch .Lpost
.LsT:
	s_sub_u32 s44, s44, 4
	s_subb_u32 s45, s45, 0
	s_setpc_b64 s[44:45]
	s_branch .LtQ
; event handlers: one barrier each (x, wq, q-poll time, q ready, K, max, P, 3/4 V, V, w_down); the word in front of each is its entry into the static tail
.LhB0:
	s_barrier
	s_waitcnt lgkmcnt(0)
	s_lshl_b32 s42, s40, 4
	s_add_i32 s42, s42, 0
	s_movk_i32 s47, 17
	s_add_u32 s44, s36, .LhB1-.Lbase
	s_addc_u32 s45, s37, 0
	s_branch .Lloop
	s_branch .LtQ
.LhB1:
	s_barrier
	s_movk_i32 s47, 43
	s_add_u32 s44, s36, .LhB2-.Lbase
	s_addc_u32 s45, s37, 0
	s_branch .Lloop
	s_branch .LtQ
.LhB2:
	s_waitcnt lgkmcnt(0)
	s_barrier
	s_movk_i32 s47, 63
	s_add_u32 s44, s36, .LhQ-.Lbase
	s_addc_u32 s45, s37, 0
	s_branch .Lloop
	s_branch .LtQ
.LhQ:
	ds_read_b32 v20, v9 offset:4
	s_waitcnt lgkmcnt(0)
	v_readfirstlane_b32 s29, v20
	s_cmp_lt_u32 s29, 9
	s_cbranch_scc1 .Lcont
	s_barrier
	s_movk_i32 s47, 65
	s_add_u32 s44, s36, .Lh3-.Lbase
	s_addc_u32 s45, s37, 0
	s_branch .Lloop
	s_branch .Lt3
.Lh3:
	s_barrier
	s_add_u32 s51, s46, 8
	s_mov_b32 s47, 0
	s_add_u32 s44, s36, .Lh4-.Lbase
	s_addc_u32 s45, s37, 0
	s_branch .Lissue
	s_branch .Lt4
.Lh4:
	s_barrier
	s_add_u32 s51, s46, 4
	s_mov_b32 s47, 0
	s_add_u32 s44, s36, .Lh5-.Lbase
	s_addc_u32 s45, s37, 0
	s_branch .Lissue
	s_branch .Lt5
.Lh5:
	s_barrier
	s_movk_i32 s47, 89
	s_add_u32 s44, s36, .Lh6a-.Lbase
	s_addc_u32 s45, s37, 0
	s_branch .Lloop
	s_branch .Lt6a
.Lh6a:
	s_barrier
	s_movk_i32 s47, 97
	s_add_u32 s44, s36, .Lh6b-.Lbase
	s_addc_u32 s45, s37, 0
	s_branch .Lloop
	s_branch .Lt6b
.Lh6b:
	s_barrier
	s_add_u32 s51, s46, 99
	s_mov_b32 s47, 0
	s_add_u32 s44, s36, .Lh6c-.Lbase
	s_addc_u32 s45, s37, 0
	s_branch .Lissue
	s_branch .Lt6c
.Lh6c:
	s_barrier
	s_movk_i32 s47, 999
	s_add_u32 s44, s36, .Lh7-.Lbase
	s_addc_u32 s45, s37, 0
	s_branch .Lloop
	s_branch .Lt7
.Lh7:
; static tail once everything is issued
.LtQ:
	s_barrier
.Lt3:
	s_waitcnt vmcnt(56)
	s_barrier
.Lt4:
	s_barrier
.Lt5:
	s_barrier
.Lt6a:
	s_waitcnt vmcnt(32)
	s_barrier
.Lt6b:
	s_waitcnt vmcnt(24)
	s_barrier
.Lt6c:
	s_waitcnt vmcnt(16)
	s_barrier
.Lt7:
	s_cmp_ge_u32 s2, 32
	s_cbranch_scc0 .Lfinal
	s_waitcnt vmcnt(0)
	s_barrier
	s_barrier
.Lexit_loader:
	s_endpgm
; WAVES 1-5: LayerNorm (bf16 rounding as the reference); waves 1-4 then q tile -> publish, later wo tile -> publish
.Lgwave:
	v_bfe_u32 v6, v3, 1, 2
	v_lshlrev_b32 v6, 5, v6
	v_lshl_add_u32 v6, v3, 7, v6
	v_add_u32 v6, v6, v4
	s_lshl_b32 s29, s3, 10
	v_add_u32 v7, s29, v5
	s_mov_b64 s[38:39], 0xffff
	s_lshr_b32 s31, s25, 3
	s_bfe_u32 s30, s2, 0x20001
	s_cmp_eq_u32 s3, 5
	s_cselect_b32 s30, s30, s31
	s_lshl_b32 s30, s30, 8
	s_add_u32 s29, s29, s30
	v_lshl_add_u32 v8, v1, 3, s29
	s_cmp_eq_u32 s3, 5
	s_cbranch_scc1 .Lauxpre
	s_sub_u32 s53, s3, 1
	s_lshl_b32 s29, s53, 5
	v_xor_b32 v6, s29, v6
	s_and_b32 s29, s25, 7
	s_lshl_b32 s29, s29, 11
	s_lshl_b32 s30, s27, 14
	s_add_u32 s29, s29, s30
	s_lshl_b32 s30, s31, 9
	s_add_u32 s29, s29, s30
	s_lshl_b32 s30, s53, 7
	s_add_u32 s29, s29, s30
	s_lshr_b32 s29, s29, 1
	v_lshl_add_u32 v40, v1, 4, s29
	s_mov_b32 s92, 0x07060501
	s_mov_b32 s93, 0x07060502
	s_mov_b32 s94, 0x07060503
	s_mov_b32 s90, 65537
	s_mov_b32 s91, s90
	s_add_u32 s34, s22, 4096
	s_addc_u32 s35, s23, 0
.Lln:
	s_barrier
	ds_read_b128 v[12:15], v5 offset:8704
	s_waitcnt lgkmcnt(0)
	v_lshlrev_b32 v16, 16, v12
	v_and_b32 v17, 0xffff0000, v12
	v_lshlrev_b32 v18, 16, v13
	v_and_b32 v19, 0xffff0000, v13
	v_lshlrev_b32 v20, 16, v14
	v_and_b32 v21, 0xffff0000, v14
	v_lshlrev_b32 v22, 16, v15
	v_and_b32 v23, 0xffff0000, v15
	v_pk_add_f32 v[24:25], v[16:17], v[18:19]
	v_pk_add_f32 v[26:27], v[20:21], v[22:23]
	v_pk_add_f32 v[24:25], v[24:25], v[26:27]
	v_add_f32 v24, v24, v25
	s_nop 1
	v_add_f32_dpp v24, v24, v24 quad_perm:[1,0,3,2] row_mask:0xf bank_mask:0xf
	s_nop 1
	v_add_f32_dpp v24, v24, v24 quad_perm:[2,3,0,1] row_mask:0xf bank_mask:0xf
	s_nop 1
	v_add_f32_dpp v24, v24, v24 row_half_mirror row_mask:0xf bank_mask:0xf
	s_nop 1
	v_add_f32_dpp v24, v24, v24 row_mirror row_mask:0xf bank_mask:0xf
	v_mov_b32 v25, v24
	s_nop 1
	v_permlane16_swap_b32 v24, v25
	v_add_f32 v24, v24, v25
	v_mov_b32 v25, v24
	s_nop 1
	v_permlane32_swap_b32 v24, v25
)ASM"
R"ASM(	v_add_f32 v24, v24, v25
	v_mul_f32 v24, 0x3b000000, v24
	v_pk_add_f32 v[28:29], v[16:17], v[24:25] op_sel_hi:[1,0] neg_lo:[0,1] neg_hi:[0,1]
	v_pk_add_f32 v[30:31], v[18:19], v[24:25] op_sel_hi:[1,0] neg_lo:[0,1] neg_hi:[0,1]
	v_pk_add_f32 v[32:33], v[20:21], v[24:25] op_sel_hi:[1,0] neg_lo:[0,1] neg_hi:[0,1]
	v_pk_add_f32 v[34:35], v[22:23], v[24:25] op_sel_hi:[1,0] neg_lo:[0,1] neg_hi:[0,1]
	v_pk_mul_f32 v[26:27], v[28:29], v[28:29]
	v_pk_fma_f32 v[26:27], v[30:31], v[30:31], v[26:27]
	v_pk_fma_f32 v[26:27], v[32:33], v[32:33], v[26:27]
	v_pk_fma_f32 v[26:27], v[34:35], v[34:35], v[26:27]
	v_add_f32 v26, v26, v27
	s_nop 1
	v_add_f32_dpp v26, v26, v26 quad_perm:[1,0,3,2] row_mask:0xf bank_mask:0xf
	s_nop 1
	v_add_f32_dpp v26, v26, v26 quad_perm:[2,3,0,1] row_mask:0xf bank_mask:0xf
	s_nop 1
	v_add_f32_dpp v26, v26, v26 row_half_mirror row_mask:0xf bank_mask:0xf
	s_nop 1
	v_add_f32_dpp v26, v26, v26 row_mirror row_mask:0xf bank_mask:0xf
	v_mov_b32 v27, v26
	s_nop 1
	v_permlane16_swap_b32 v26, v27
	v_add_f32 v26, v26, v27
	v_mov_b32 v27, v26
	s_nop 1
	v_permlane32_swap_b32 v26, v27
	v_add_f32 v26, v26, v27
	v_mul_f32 v26, 0x3b000000, v26
	v_cvt_pk_bf16_f32 v24, v24, v26
	v_and_b32 v26, 0xffff0000, v24
	v_lshlrev_b32 v24, 16, v24
	v_add_f32 v26, 0x3727c5ac, v26
	v_cvt_pk_bf16_f32 v26, v26, v26
	v_lshlrev_b32 v26, 16, v26
	v_sqrt_f32 v26, v26
	v_pk_add_f32 v[16:17], v[16:17], v[24:25] op_sel_hi:[1,0] neg_lo:[0,1] neg_hi:[0,1]
	v_pk_add_f32 v[18:19], v[18:19], v[24:25] op_sel_hi:[1,0] neg_lo:[0,1] neg_hi:[0,1]
	v_pk_add_f32 v[20:21], v[20:21], v[24:25] op_sel_hi:[1,0] neg_lo:[0,1] neg_hi:[0,1]
	v_pk_add_f32 v[22:23], v[22:23], v[24:25] op_sel_hi:[1,0] neg_lo:[0,1] neg_hi:[0,1]
	v_cvt_pk_bf16_f32 v26, v26, v26
	v_lshlrev_b32 v26, 16, v26
	v_rcp_f32 v26, v26
	v_cvt_pk_bf16_f32 v12, v16, v17
	v_cvt_pk_bf16_f32 v13, v18, v19
	v_cvt_pk_bf16_f32 v14, v20, v21
	v_cvt_pk_bf16_f32 v15, v22, v23
	v_lshlrev_b32 v16, 16, v12
	v_and_b32 v17, 0xffff0000, v12
	v_lshlrev_b32 v18, 16, v13
	v_and_b32 v19, 0xffff0000, v13
	v_lshlrev_b32 v20, 16, v14
	v_and_b32 v21, 0xffff0000, v14
	v_lshlrev_b32 v22, 16, v15
	v_and_b32 v23, 0xffff0000, v15
	v_pk_mul_f32 v[16:17], v[16:17], v[26:27] op_sel_hi:[1,0]
	v_pk_mul_f32 v[18:19], v[18:19], v[26:27] op_sel_hi:[1,0]
	v_pk_mul_f32 v[20:21], v[20:21], v[26:27] op_sel_hi:[1,0]
	v_pk_mul_f32 v[22:23], v[22:23], v[26:27] op_sel_hi:[1,0]
	v_cvt_pk_bf16_f32 v12, v16, v17
	v_cvt_pk_bf16_f32 v13, v18, v19
	v_cvt_pk_bf16_f32 v14, v20, v21
	v_cvt_pk_bf16_f32 v15, v22, v23
	ds_write_b128 v7, v[12:15]
	ds_read2_b64 v[16:19], v8 offset0:0 offset1:4
	ds_read2_b64 v[20:23], v8 offset0:8 offset1:12
	ds_read2_b64 v[24:27], v8 offset0:16 offset1:20
	ds_read2_b64 v[28:31], v8 offset0:24 offset1:28
	s_cmp_eq_u32 s3, 5
	s_cbranch_scc1 .Laux
	v_mov_b32 v12, v40
	s_waitcnt lgkmcnt(0)
	s_barrier
	ds_read_b32 v63, v9 offset:12
	ds_read_b64_tr_b16 v[32:33], v6 offset:13312
	ds_read_b64_tr_b16 v[34:35], v6 offset:15360
	ds_read_b64_tr_b16 v[36:37], v6 offset:17408
	ds_read_b64_tr_b16 v[38:39], v6 offset:19456
	ds_read_b64_tr_b16 v[40:41], v6 offset:21504
	ds_read_b64_tr_b16 v[42:43], v6 offset:23552
	ds_read_b64_tr_b16 v[44:45], v6 offset:25600
	ds_read_b64_tr_b16 v[46:47], v6 offset:27648
	s_waitcnt lgkmcnt(6)
	v_mfma_f32_16x16x32_bf16 v[48:51], v[32:35], v[16:19], 0
	s_waitcnt lgkmcnt(4)
	v_mfma_f32_16x16x32_bf16 v[48:51], v[36:39], v[20:23], v[48:51]
	s_waitcnt lgkmcnt(2)
	v_mfma_f32_16x16x32_bf16 v[48:51], v[40:43], v[24:27], v[48:51]
	s_waitcnt lgkmcnt(0)
	v_mfma_f32_16x16x32_bf16 v[48:51], v[44:47], v[28:31], v[48:51]
	s_mov_b64 exec, s[90:91]
	s_nop 6
	v_add_u32 v48, 0x80, v48
	v_add_u32 v49, 0x80, v49
	v_add_u32 v50, 0x80, v50
	v_add_u32 v51, 0x80, v51
	v_perm_b32 v48, v48, v51, s92
	v_perm_b32 v49, v49, v51, s93
	v_perm_b32 v50, v50, v51, s94
	v_bitop3_b32 v51, v48, v49, v50 bitop3:0x96
	v_xor_b32 v51, v63, v51
	global_store_dwordx4 v12, v[48:51], s[34:35] sc1
	s_mov_b64 exec, s[36:37]
	s_branch .Lpo7
.Lpad7:
	s_branch .Lpr7
.Lpo7:
	v_add_u32 v11, 119808, v6
	s_and_b32 s29, s25, 7
	s_lshl_b32 s29, s29, 2
	s_add_u32 s29, s29, s53
	s_lshl_b32 s29, s29, 5
	s_lshl_b32 s30, s27, 2
	s_add_u32 s29, s29, s30
	s_add_u32 s29, s29, s31
	s_lshl_b32 s29, s29, 7
	v_lshl_add_u32 v12, v0, 3, s29
	s_add_u32 s34, s22, 790528
	s_addc_u32 s35, s23, 0
	v_mul_u32_u24 v13, 272, v2
	v_lshl_add_u32 v13, v1, 4, v13
	v_lshlrev_b32 v14, 5, v1
	s_cmp_lg_u32 s3, 1
	s_cbranch_scc1 .Lltd
	s_branch .Lpad1
.Lpr1:
	s_branch .Lpad7
.Lpr7:
	s_branch .Lpad9
.Lpr9:
	s_branch .Lpad10
.Lpr10:
	s_branch .Lpad13
.Lpr13:
	s_branch .Lpad14
.Lpr14:
	s_branch .Lpad16
.Lpr16:
.Lltd:
	s_barrier
	s_mov_b64 exec, 1
	global_load_dword v66, v12, s[34:35]
	s_mov_b64 exec, -1
	s_cmp_lg_u32 s3, 2
	s_cbranch_scc1 .Lopw
	s_cmp_ge_u32 s2, 32
	s_cbranch_scc1 .Lopw
	s_lshl_b32 s29, s2, 5
	v_lshl_add_u32 v50, v0, 2, s29
	s_mov_b64 exec, 0xff
	global_store_dword v50, v9, s[20:21] sc1 
	s_mov_b64 exec, -1
.Lopw:
	s_barrier
	s_barrier
	s_barrier
	s_barrier
	s_lshl4_add_u32 s29, s2, s3
	s_cmp_lg_u32 s29, 3
	s_cbranch_scc1 .Leaw
	s_mov_b64 exec, 1
	global_atomic_add v9, v9, s[22:23] sc1
	s_mov_b64 exec, -1
.Leaw:
	s_barrier
	s_barrier
	s_barrier
	s_barrier
	ds_read_b64_tr_b16 v[32:33], v11 offset:0
	ds_read_b64_tr_b16 v[34:35], v11 offset:2048
	ds_read_b64_tr_b16 v[36:37], v11 offset:4096
	ds_read_b64_tr_b16 v[38:39], v11 offset:6144
	ds_read_b64_tr_b16 v[40:41], v11 offset:8192
	ds_read_b64_tr_b16 v[42:43], v11 offset:10240
	ds_read_b64_tr_b16 v[44:45], v11 offset:12288
	ds_read_b64_tr_b16 v[46:47], v11 offset:14336
	s_waitcnt vmcnt(0) lgkmcnt(0)
	s_barrier
	ds_read_b128 v[16:19], v13 offset:8704
	ds_read_b128 v[20:23], v13 offset:8768
	ds_read_b128 v[24:27], v13 offset:8832
	ds_read_b128 v[28:31], v13 offset:8896
	ds_read_b32 v60, v9 offset:8
	ds_read_b128 v[52:55], v14 offset:13056
	ds_read_b128 v[56:59], v14 offset:13072
	s_waitcnt lgkmcnt(6)
	v_mfma_f32_16x16x32_bf16 v[48:51], v[16:19], v[32:35], 0
	s_waitcnt lgkmcnt(5)
	v_mfma_f32_16x16x32_bf16 v[48:51], v[20:23], v[36:39], v[48:51]
	s_waitcnt lgkmcnt(4)
	v_mfma_f32_16x16x32_bf16 v[48:51], v[24:27], v[40:43], v[48:51]
	s_waitcnt lgkmcnt(3)
	v_mfma_f32_16x16x32_bf16 v[48:51], v[28:31], v[44:47], v[48:51]
	s_waitcnt lgkmcnt(1)
	v_sub_f32 v52, v52, v60
	v_sub_f32 v54, v54, v60
	v_exp_f32 v52, v52
	v_exp_f32 v54, v54
	s_waitcnt lgkmcnt(0)
	v_sub_f32 v56, v56, v60
	v_sub_f32 v58, v58, v60
	v_exp_f32 v56, v56
	v_exp_f32 v58, v58
	v_mul_f32 v61, v52, v53
	v_fmac_f32 v61, v54, v55
	v_fmac_f32 v61, v56, v57
	v_fmac_f32 v61, v58, v59
	v_mul_f32 v62, v52, v48
	v_fmac_f32 v62, v54, v49
	v_fmac_f32 v62, v56, v50
	v_fmac_f32 v62, v58, v51
	s_nop 1
	v_permlane32_swap_b32 v62, v61
	v_add_f32 v62, v62, v61
	v_mov_b32 v61, v62
	s_nop 1
	v_permlane16_swap_b32 v62, v61
	v_add_f32 v62, v62, v61
	v_rcp_f32 v61, v62
	s_nop 1
	v_permlane32_swap_b32 v61, v60
	s_mov_b64 exec, s[38:39]
	v_mul_f32 v62, v62, v60
	global_store_dwordx2 v12, v[62:63], s[34:35] sc1
	s_barrier
	s_endpgm
; WAVES 6-13: gather q, scores, softmax, P.V, publish {bf16 o, lse}, gather the 32 partials of one head, merge
.Lworker:
	s_sub_u32 s52, s3, 6
	v_mov_b32 v12, 0xff800000
	v_mov_b32 v13, v12
	v_mov_b32 v14, v12
	v_mov_b32 v15, v12
	ds_write_b128 v9, v[12:15] offset:16
	ds_write_b32 v9, v12 offset:8
	ds_write_b32 v9, v9 offset:4
	s_barrier
	s_memrealtime s[50:51]
	s_lshl_b32 s29, s24, 13
	s_lshl_b32 s30, s52, 10
	s_add_u32 s29, s29, s30
	v_add_u32 v5, s29, v5
	s_mov_b32 s92, 0x04000c0c
	s_mov_b32 s93, 0x0706000c
	s_add_u32 s34, s22, 4096
	s_addc_u32 s35, s23, 0
	s_mov_b32 s40, 0x000f000f
	s_mov_b32 s41, s40
	s_mov_b32 s42, 1
	s_mov_b32 s43, 1
	s_mov_b32 s44, 0x3fb8aa3b
	s_mov_b32 s45, 0x3db504f3
	s_lshr_b32 s31, s52, 1
	s_and_b32 s53, s52, 1
	s_mul_i32 s29, s31, 320
	s_lshl_b32 s30, s53, 7
	s_add_u32 s29, s29, s30
	v_and_b32 v10, 31, v0
	v_lshrrev_b32 v11, 5, v0
	v_lshl_add_u32 v6, v2, 3, s29
	v_lshl_add_u32 v6, v1, 1, v6
	s_lshl_b32 s29, s52, 12
	s_add_u32 s29, s29, 46080
	v_xor_b32 v64, v1, v2
	v_lshlrev_b32 v64, 4, v64
	v_lshl_add_u32 v64, v2, 8, v64
	v_add_u32 v64, s29, v64
	v_xor_b32 v65, 64, v64
	v_xor_b32 v66, 0x80, v64
	v_xor_b32 v67, 0xc0, v64
	v_and_b32 v68, 3, v0
	v_mul_u32_u24 v7, 320, v68
	v_lshl_add_u32 v7, v1, 4, v7
	s_lshl_b32 s29, s31, 6
	s_lshl_b32 s30, s53, 3
	s_add_u32 s29, s29, s30
	v_add_u32 v69, s29, v7
	s_lshl_b32 s29, s24, 16
	s_lshl_b32 s30, s25, 9
	s_add_u32 s29, s29, s30
	s_lshl_b32 s30, s52, 7
	s_add_u32 s29, s29, s30
	v_lshlrev_b32 v70, 14, v68
	v_lshl_add_u32 v70, v1, 4, v70
	v_add_u32 v70, s29, v70
	v_and_b32 v12, 4, v0
	v_lshl_add_u32 v70, v12, 4, v70
	v_cmp_ne_u32 s[46:47], 0, v12
	s_mov_b32 s48, 0x00ff00ff
	s_mov_b32 s49, s48
	v_lshlrev_b32 v68, 2, v68
	s_lshl_b32 s30, s52, 1
	s_and_b32 s30, s30, 6
	v_and_b32 v71, 7, v3
	v_xor_b32 v71, s30, v71
	v_lshlrev_b32 v71, 5, v71
	v_lshl_add_u32 v71, v3, 8, v71
	s_mov_b32 s29, 78848
	v_add3_u32 v71, v71, v4, s29
	v_xor_b32 v75, 32, v71
	s_lshr_b32 s29, s25, 3
	s_lshl_b32 s30, s27, 2
	s_add_u32 s29, s29, s30
	s_lshl_b32 s29, s29, 14
	s_lshl_b32 s30, s52, 11
	s_add_u32 s29, s29, s30
	v_lshl_add_u32 v72, v0, 4, s29
	s_lshl_b32 s29, s52, 1
	v_add_u32 v74, s29, v11
	v_mul_u32_u24 v73, 272, v74
	v_lshlrev_b32 v74, 3, v74
	v_lshrrev_b32 v12, 3, v10
	v_lshl_add_u32 v73, v12, 6, v73
	v_and_b32 v12, 3, v10
	v_lshl_add_u32 v73, v12, 4, v73
	v_bfe_u32 v12, v10, 2, 1
	v_lshl_add_u32 v73, v12, 3, v73
	v_mov_b32 v76, 0x3f803f80
	v_mov_b32 v77, v76
	v_mov_b32 v78, v76
	v_mov_b32 v79, v76
	s_add_u32 s54, s22, 266240
	s_addc_u32 s55, s23, 0
	s_waitcnt lgkmcnt(0)
	s_add_u32 s26, s26, 1
	s_max_u32 s26, s26, 1
	v_mov_b32 v12, s26
	ds_write_b32 v9, v12 offset:12
	s_waitcnt lgkmcnt(0)
	s_barrier
	s_barrier
	global_load_dwordx4 v[12:15], v5, s[34:35] sc1
	s_branch .Lck1
.Lpad9:
	s_branch .Lpr9
.Lrt2:
	global_load_dwordx4 v[12:15], v5, s[34:35] sc1
	s_sub_u32 s33, s33, 1
	s_cbranch_scc1 .Ldn3
.Lck1:
	s_waitcnt vmcnt(0)
.Lck1b:
	v_bitop3_b32 v20, v12, v13, v14 bitop3:0x96
	v_bitop3_b32 v20, v20, v15, s26 bitop3:0x96
	s_nop 1
	v_or_b32_dpp v20, v20, v20 quad_perm:[1,0,3,2] row_mask:0xf bank_mask:0xf
	v_cmpx_ne_u32 vcc, 0, v20
	s_cbranch_execnz .Lrt2
.Ldn3:
	s_mov_b64 exec, s[36:37]
	v_perm_b32 v16, v14, v13, s92
	v_perm_b32 v15, v16, v12, s93
	v_and_b32 v12, 0xffffff00, v12
	v_and_b32 v13, 0xffffff00, v13
	v_and_b32 v14, 0xffffff00, v14
	s_nop 1
	v_permlane32_swap_b32 v12, v14
	v_permlane32_swap_b32 v13, v15
	v_add_f32 v12, v12, v14
	v_add_f32 v13, v13, v15
	s_nop 1
	v_permlane16_swap_b32 v12, v13
	v_add_f32 v12, v12, v13
	v_cvt_pk_bf16_f32 v12, v12, v12
	ds_write_b16 v6, v12 offset:6144
	v_mov_b32 v21, 1
	s_mov_b64 exec, 1
	ds_add_u32 v9, v21 offset:4
	s_mov_b64 exec, s[36:37]
	s_waitcnt lgkmcnt(0)
	s_barrier
	ds_read_b128 v[32:35], v7 offset:6144
	ds_read_b128 v[36:39], v7 offset:6208
	ds_read_b128 v[40:43], v7 offset:6272
	ds_read_b128 v[44:47], v7 offset:6336
	s_barrier
	ds_read_b128 v[80:83], v64
	ds_read_b128 v[84:87], v65
	ds_read_b128 v[88:91], v66
	ds_read_b128 v[92:95], v67
	s_waitcnt lgkmcnt(3)
	v_mfma_f32_16x16x32_bf16 v[48:51], v[80:83], v[32:35], 0
	s_waitcnt lgkmcnt(2)
	v_mfma_f32_16x16x32_bf16 v[48:51], v[84:87], v[36:39], v[48:51]
	s_waitcnt lgkmcnt(1)
	v_mfma_f32_16x16x32_bf16 v[48:51], v[88:91], v[40:43], v[48:51]
	s_waitcnt lgkmcnt(0)
	v_mfma_f32_16x16x32_bf16 v[48:51], v[92:95], v[44:47], v[48:51]
	s_mov_b64 exec, s[40:41]
	s_nop 6
	v_cvt_pk_bf16_f32 v52, v48, v49
	v_cvt_pk_bf16_f32 v53, v50, v51
	v_lshlrev_b32 v48, 16, v52
	v_and_b32 v49, 0xffff0000, v52
	v_lshlrev_b32 v50, 16, v53
	v_and_b32 v51, 0xffff0000, v53
	v_pk_mul_f32 v[48:49], v[48:49], s[44:45] op_sel:[0,1] op_sel_hi:[1,1]
	v_pk_mul_f32 v[50:51], v[50:51], s[44:45] op_sel:[0,1] op_sel_hi:[1,1]
	v_cvt_pk_bf16_f32 v52, v48, v49
	v_cvt_pk_bf16_f32 v53, v50, v51
	v_lshlrev_b32 v48, 16, v52
	v_and_b32 v49, 0xffff0000, v52
	v_lshlrev_b32 v50, 16, v53
	v_and_b32 v51, 0xffff0000, v53
	v_max3_f32 v52, v48, v49, v50
	v_max_f32 v52, v52, v51
	ds_max_f32 v68, v52 offset:16
	s_waitcnt lgkmcnt(0)
	s_barrier
	s_mov_b64 exec, s[36:37]
)ASM"
R"ASM(	ds_read_b32 v54, v68 offset:16
	s_waitcnt lgkmcnt(0)
	v_mul_f32 v54, s44, v54
	s_mov_b64 exec, s[40:41]
	v_fma_f32 v48, v48, s44, -v54
	v_fma_f32 v49, v49, s44, -v54
	v_fma_f32 v50, v50, s44, -v54
	v_fma_f32 v51, v51, s44, -v54
	v_exp_f32 v48, v48
	v_exp_f32 v49, v49
	v_exp_f32 v50, v50
	v_exp_f32 v51, v51
	s_nop 0
	v_cvt_pk_bf16_f32 v52, v48, v49
	v_cvt_pk_bf16_f32 v53, v50, v51
	ds_write_b64 v69, v[52:53] offset:7424
	s_mov_b64 exec, s[36:37]
	s_cmp_ge_u32 s52, 4
	s_cbranch_scc1 .Lnopv
	s_waitcnt lgkmcnt(0)
	s_barrier
	ds_read_b128 v[32:35], v7 offset:7424
	ds_read_b128 v[36:39], v7 offset:7488
	ds_read_b128 v[40:43], v7 offset:7552
	ds_read_b128 v[44:47], v7 offset:7616
	s_barrier
	ds_read_b64_tr_b16 v[16:17], v71 offset:0
	ds_read_b64_tr_b16 v[18:19], v71 offset:4096
	ds_read_b64_tr_b16 v[80:81], v75 offset:0
	ds_read_b64_tr_b16 v[82:83], v75 offset:4096
	ds_read_b64_tr_b16 v[20:21], v71 offset:8192
	ds_read_b64_tr_b16 v[22:23], v71 offset:12288
	ds_read_b64_tr_b16 v[84:85], v75 offset:8192
	ds_read_b64_tr_b16 v[86:87], v75 offset:12288
	s_waitcnt lgkmcnt(11)
	v_mfma_f32_16x16x32_bf16 v[56:59], v[76:79], v[32:35], 0
	ds_read_b64_tr_b16 v[24:25], v71 offset:16384
	ds_read_b64_tr_b16 v[26:27], v71 offset:20480
	ds_read_b64_tr_b16 v[88:89], v75 offset:16384
	ds_read_b64_tr_b16 v[90:91], v75 offset:20480
	s_waitcnt lgkmcnt(14)
	v_mfma_f32_16x16x32_bf16 v[56:59], v[76:79], v[36:39], v[56:59]
	s_waitcnt lgkmcnt(13)
	v_mfma_f32_16x16x32_bf16 v[56:59], v[76:79], v[40:43], v[56:59]
	s_waitcnt lgkmcnt(12)
	v_mfma_f32_16x16x32_bf16 v[56:59], v[76:79], v[44:47], v[56:59]
	s_waitcnt lgkmcnt(10)
	v_mfma_f32_16x16x32_bf16 v[48:51], v[16:19], v[32:35], 0
	s_waitcnt lgkmcnt(8)
	v_mfma_f32_16x16x32_bf16 v[12:15], v[80:83], v[32:35], 0
	s_waitcnt lgkmcnt(6)
	v_mfma_f32_16x16x32_bf16 v[48:51], v[20:23], v[36:39], v[48:51]
	s_waitcnt lgkmcnt(4)
	v_mfma_f32_16x16x32_bf16 v[12:15], v[84:87], v[36:39], v[12:15]
	v_log_f32 v62, v56
	v_rcp_f32 v56, v56
	s_waitcnt lgkmcnt(2)
	v_mfma_f32_16x16x32_bf16 v[48:51], v[24:27], v[40:43], v[48:51]
	s_waitcnt lgkmcnt(0)
	v_mfma_f32_16x16x32_bf16 v[12:15], v[88:91], v[40:43], v[12:15]
	v_add_f32 v62, v62, v54
	v_xor_b32 v55, s26, v62
	s_barrier
	ds_read_b64_tr_b16 v[28:29], v71 offset:24576
	ds_read_b64_tr_b16 v[30:31], v71 offset:28672
	ds_read_b64_tr_b16 v[92:93], v75 offset:24576
	ds_read_b64_tr_b16 v[94:95], v75 offset:28672
	s_waitcnt lgkmcnt(2)
	v_mfma_f32_16x16x32_bf16 v[48:51], v[28:31], v[44:47], v[48:51]
	s_waitcnt lgkmcnt(0)
	v_mfma_f32_16x16x32_bf16 v[12:15], v[92:95], v[44:47], v[12:15]
	s_mov_b64 exec, s[48:49]
	s_nop 6
	v_cndmask_b32 v48, v48, v12, s[46:47]
	v_cndmask_b32 v49, v49, v13, s[46:47]
	v_cndmask_b32 v50, v50, v14, s[46:47]
	v_cndmask_b32 v51, v51, v15, s[46:47]
	v_pk_mul_f32 v[48:49], v[48:49], v[56:57] op_sel_hi:[1,0]
	v_pk_mul_f32 v[50:51], v[50:51], v[56:57] op_sel_hi:[1,0]
	v_cvt_pk_bf16_f32 v60, v48, v49
	v_cvt_pk_bf16_f32 v61, v50, v51
	v_bitop3_b32 v63, v60, v61, v55 bitop3:0x96
	global_store_dwordx4 v70, v[60:63], s[54:55] sc1
	s_mov_b64 exec, s[36:37]
.Lpvdone:
	s_barrier
.Ltau:
	s_memrealtime s[90:91]
	s_waitcnt lgkmcnt(0)
	s_sub_u32 s90, s90, s50
	s_cmp_lt_u32 s90, 431
	s_cbranch_scc1 .Ltau
	global_load_dwordx4 v[12:15], v72, s[54:55] sc1
	global_load_dwordx4 v[16:19], v72, s[54:55] offset:1024 sc1
	s_barrier
	s_branch .LBck
.Lpad10:
	s_branch .Lpr10
.LBrt:
	s_mov_b64 exec, vcc
	global_load_dwordx4 v[12:15], v72, s[54:55] sc1
	global_load_dwordx4 v[16:19], v72, s[54:55] offset:1024 sc1
	s_sub_u32 s33, s33, 1
	s_cbranch_scc1 .LBdn
.LBck:
	s_waitcnt vmcnt(0)
	v_bitop3_b32 v20, v12, v13, v14 bitop3:0x96
	v_bitop3_b32 v21, v16, v17, v18 bitop3:0x96
	v_xor_b32 v20, v20, v15
	v_xor_b32 v21, v21, v19
	v_bitop3_b32 v20, v20, v21, s26 bitop3:0x7e
	v_max_f32 v22, v14, v18
	v_sub_f32 v32, v14, v22
	v_or_b32_dpp v20, v20, v20 quad_perm:[1,0,3,2] row_mask:0xf bank_mask:0xf
	v_cmp_ne_u32 vcc, 0, v20
	v_sub_f32 v34, v18, v22
	v_exp_f32 v32, v32
	v_exp_f32 v34, v34
	v_lshlrev_b32 v24, 16, v12
	v_and_b32 v25, 0xffff0000, v12
	s_cbranch_vccnz .LBrt
.LBdn:
	s_mov_b64 exec, s[36:37]
	v_lshlrev_b32 v26, 16, v13
	v_and_b32 v27, 0xffff0000, v13
	v_lshlrev_b32 v28, 16, v16
	v_and_b32 v29, 0xffff0000, v16
	v_lshlrev_b32 v30, 16, v17
	v_and_b32 v31, 0xffff0000, v17
	v_pk_mul_f32 v[24:25], v[24:25], v[32:33] op_sel_hi:[1,0]
	v_pk_mul_f32 v[26:27], v[26:27], v[32:33] op_sel_hi:[1,0]
	v_pk_fma_f32 v[24:25], v[28:29], v[34:35], v[24:25] op_sel_hi:[1,0,1]
	v_pk_fma_f32 v[26:27], v[30:31], v[34:35], v[26:27] op_sel_hi:[1,0,1]
	v_cvt_pk_bf16_f32 v24, v24, v25
	v_cvt_pk_bf16_f32 v25, v26, v27
	ds_write_b64 v73, v[24:25] offset:8704
	v_add_f32 v23, v32, v34
	s_mov_b64 exec, s[42:43]
	ds_write_b64 v74, v[22:23] offset:13056
	ds_max_f32 v9, v22 offset:8
	s_waitcnt lgkmcnt(0)
	s_barrier
	s_cmp_ge_u32 s2, 32
	s_cbranch_scc1 .Lwend
	s_mov_b64 exec, s[36:37]
	s_add_u32 s34, s22, 790528
	s_addc_u32 s35, s23, 0
	s_lshl_b32 s29, s2, 12
	s_lshl_b32 s30, s52, 9
	s_add_u32 s29, s29, s30
	v_lshl_add_u32 v40, v0, 3, s29
	v_mov_b32 v42, 0
	v_mov_b32 v43, 0
	v_mul_u32_u24 v41, 144, v2
	v_lshl_add_u32 v41, v1, 2, v41
	s_lshl_b32 s29, s52, 4
	v_add_u32 v41, s29, v41
	s_sleep 6
	v_mov_b32 v38, s26
	global_atomic_add_x2 v[44:45], v40, v[42:43], s[34:35] sc0 sc1
	s_sleep 4
	global_atomic_add_x2 v[46:47], v40, v[42:43], s[34:35] sc0 sc1
	s_sleep 4
; workgroups 0..31: read the output partials with returning device atomics (add 0), three samples in flight
.Lat:
	global_atomic_add_x2 v[48:49], v40, v[42:43], s[34:35] sc0 sc1
	s_sub_u32 s33, s33, 1
	s_cbranch_scc1 .Latd
	s_waitcnt vmcnt(2)
	ds_write_b32 v41, v44 offset:6144
	v_xor_b32_dpp v39, v45, v45 quad_perm:[1,0,3,2] row_mask:0xf bank_mask:0xf
	v_bitop3_b32 v39, v39, v45, s26 bitop3:0xf6
	v_cmpx_ne_u32 vcc, 0, v39
	s_cbranch_execz .Latd
	global_atomic_add_x2 v[44:45], v40, v[42:43], s[34:35] sc0 sc1
	s_nop 1
	s_waitcnt vmcnt(2)
	ds_write_b32 v41, v46 offset:6144
	v_xor_b32_dpp v39, v47, v47 quad_perm:[1,0,3,2] row_mask:0xf bank_mask:0xf
	v_bitop3_b32 v39, v39, v47, s26 bitop3:0xf6
	v_cmpx_ne_u32 vcc, 0, v39
	s_cbranch_execz .Latd
	global_atomic_add_x2 v[46:47], v40, v[42:43], s[34:35] sc0 sc1
	s_nop 1
	s_waitcnt vmcnt(2)
	ds_write_b32 v41, v48 offset:6144
	v_xor_b32_dpp v39, v49, v49 quad_perm:[1,0,3,2] row_mask:0xf bank_mask:0xf
	v_bitop3_b32 v39, v39, v49, s26 bitop3:0xf6
	v_cmpx_ne_u32 vcc, 0, v39
	s_cbranch_execnz .Lat
.Latd:
	s_waitcnt lgkmcnt(0)
	s_barrier
	s_or_b32 s29, s2, s52
	s_cbranch_scc1 .Lwend
	s_mov_b64 exec, 1
	global_store_dword v9, v38, s[22:23] sc1
.Lwend:
	s_endpgm
; workers 4-7 skip P.V
.Lnopv:
	s_waitcnt lgkmcnt(0)
	s_barrier
	s_barrier
	s_barrier
	s_branch .Lpvdone
; WAVE 5: gate/up tile -> publish; gather, gelu(gate)*up, w_down tile -> publish
.Lauxpre:
	v_xor_b32 v64, 0, v6
	v_add_u32 v68, 111616, v64
	v_add_u32 v64, 29696, v64
	v_xor_b32 v65, 32, v6
	v_add_u32 v69, 111616, v65
	v_add_u32 v65, 29696, v65
	v_xor_b32 v66, 64, v6
	v_add_u32 v70, 111616, v66
	v_add_u32 v66, 29696, v66
	v_xor_b32 v67, 96, v6
	v_add_u32 v71, 111616, v67
	v_add_u32 v67, 29696, v67
	s_lshl_b32 s29, s2, 8
	v_lshl_add_u32 v87, v2, 6, s29
	v_lshl_add_u32 v87, v1, 4, v87
	s_mov_b32 s92, 0x07060501
	s_mov_b32 s93, 0x07060502
	s_mov_b32 s94, 0x07060503
	v_cmp_eq_u32 s[40:41], 1, v2
	v_cmp_eq_u32 s[44:45], 2, v2
	v_cmp_eq_u32 s[90:91], 3, v2
	s_add_u32 s34, s22, 135168
	s_addc_u32 s35, s23, 0
	v_cmp_eq_u32 s[46:47], 1, v1
	v_cmp_eq_u32 s[48:49], 2, v1
	v_cmp_eq_u32 s[50:51], 3, v1
	s_branch .Lln
.Lpad13:
	s_branch .Lpr13
.Laux:
	v_mov_b32 v12, v87
	s_barrier
	ds_read_b32 v89, v9 offset:12
	s_waitcnt lgkmcnt(0)
	v_readfirstlane_b32 s26, v89
	s_barrier
	s_cmpk_ge_u32 s2, 224
	s_cbranch_scc1 .Laux2
	ds_read_b64_tr_b16 v[32:33], v64 offset:0
	ds_read_b64_tr_b16 v[34:35], v64 offset:2048
	ds_read_b64_tr_b16 v[36:37], v64 offset:4096
	ds_read_b64_tr_b16 v[38:39], v64 offset:6144
	ds_read_b64_tr_b16 v[40:41], v65 offset:0
	ds_read_b64_tr_b16 v[42:43], v65 offset:2048
	ds_read_b64_tr_b16 v[44:45], v65 offset:4096
	ds_read_b64_tr_b16 v[46:47], v65 offset:6144
	ds_read_b64_tr_b16 v[48:49], v66 offset:0
	ds_read_b64_tr_b16 v[50:51], v66 offset:2048
	ds_read_b64_tr_b16 v[52:53], v66 offset:4096
	ds_read_b64_tr_b16 v[54:55], v66 offset:6144
	ds_read_b64_tr_b16 v[56:57], v67 offset:0
	ds_read_b64_tr_b16 v[58:59], v67 offset:2048
	ds_read_b64_tr_b16 v[60:61], v67 offset:4096
	ds_read_b64_tr_b16 v[62:63], v67 offset:6144
	s_waitcnt lgkmcnt(0)
	v_mfma_f32_16x16x32_bf16 v[72:75], v[32:35], v[16:19], 0
	v_mfma_f32_16x16x32_bf16 v[76:79], v[40:43], v[16:19], 0
	v_mfma_f32_16x16x32_bf16 v[80:83], v[48:51], v[16:19], 0
	v_mfma_f32_16x16x32_bf16 v[84:87], v[56:59], v[16:19], 0
	v_mfma_f32_16x16x32_bf16 v[72:75], v[36:39], v[20:23], v[72:75]
	v_mfma_f32_16x16x32_bf16 v[76:79], v[44:47], v[20:23], v[76:79]
	v_mfma_f32_16x16x32_bf16 v[80:83], v[52:55], v[20:23], v[80:83]
	v_mfma_f32_16x16x32_bf16 v[84:87], v[60:63], v[20:23], v[84:87]
	ds_read_b64_tr_b16 v[32:33], v64 offset:8192
	ds_read_b64_tr_b16 v[34:35], v64 offset:10240
	ds_read_b64_tr_b16 v[36:37], v64 offset:12288
	ds_read_b64_tr_b16 v[38:39], v64 offset:14336
	ds_read_b64_tr_b16 v[40:41], v65 offset:8192
	ds_read_b64_tr_b16 v[42:43], v65 offset:10240
	ds_read_b64_tr_b16 v[44:45], v65 offset:12288
	ds_read_b64_tr_b16 v[46:47], v65 offset:14336
	ds_read_b64_tr_b16 v[48:49], v66 offset:8192
	ds_read_b64_tr_b16 v[50:51], v66 offset:10240
	ds_read_b64_tr_b16 v[52:53], v66 offset:12288
	ds_read_b64_tr_b16 v[54:55], v66 offset:14336
	ds_read_b64_tr_b16 v[56:57], v67 offset:8192
	ds_read_b64_tr_b16 v[58:59], v67 offset:10240
	ds_read_b64_tr_b16 v[60:61], v67 offset:12288
	ds_read_b64_tr_b16 v[62:63], v67 offset:14336
	s_waitcnt lgkmcnt(0)
	v_mfma_f32_16x16x32_bf16 v[72:75], v[32:35], v[24:27], v[72:75]
	v_mfma_f32_16x16x32_bf16 v[76:79], v[40:43], v[24:27], v[76:79]
	v_mfma_f32_16x16x32_bf16 v[80:83], v[48:51], v[24:27], v[80:83]
	v_mfma_f32_16x16x32_bf16 v[84:87], v[56:59], v[24:27], v[84:87]
	v_mfma_f32_16x16x32_bf16 v[72:75], v[36:39], v[28:31], v[72:75]
	v_mfma_f32_16x16x32_bf16 v[76:79], v[44:47], v[28:31], v[76:79]
	v_mfma_f32_16x16x32_bf16 v[80:83], v[52:55], v[28:31], v[80:83]
	v_mfma_f32_16x16x32_bf16 v[84:87], v[60:63], v[28:31], v[84:87]
	s_nop 7
	v_cndmask_b32 v72, v72, v76, s[40:41]
	v_cndmask_b32 v73, v73, v77, s[40:41]
	v_cndmask_b32 v74, v74, v78, s[40:41]
	v_cndmask_b32 v75, v75, v79, s[40:41]
	v_cndmask_b32 v72, v72, v80, s[44:45]
	v_cndmask_b32 v73, v73, v81, s[44:45]
	v_cndmask_b32 v74, v74, v82, s[44:45]
	v_cndmask_b32 v75, v75, v83, s[44:45]
	v_cndmask_b32 v72, v72, v84, s[90:91]
	v_cndmask_b32 v73, v73, v85, s[90:91]
	v_cndmask_b32 v74, v74, v86, s[90:91]
	v_cndmask_b32 v75, v75, v87, s[90:91]
	v_add_u32 v72, 0x80, v72
	v_add_u32 v73, 0x80, v73
	v_add_u32 v74, 0x80, v74
	v_add_u32 v75, 0x80, v75
	v_perm_b32 v72, v72, v75, s92
	v_perm_b32 v73, v73, v75, s93
	v_perm_b32 v74, v74, v75, s94
	v_bitop3_b32 v75, v72, v73, v74 bitop3:0x96
	v_xor_b32 v75, s26, v75
	s_mov_b32 exec_lo, 0x000f000f
	s_mov_b32 exec_hi, 0x000f000f
	global_store_dwordx4 v12, v[72:75], s[34:35] sc1
	s_mov_b64 exec, s[36:37]
.Laux2:
	v_mov_b32 v21, 1
	s_mov_b64 exec, 1
	ds_add_u32 v9, v21 offset:4
	s_mov_b64 exec, s[36:37]
	s_cmp_eq_u32 s24, 7
	s_cbranch_scc1 .Laux_idle
	s_lshr_b32 s31, s25, 3
	s_lshl_b32 s30, s24, 2
	s_add_u32 s31, s31, s30
	s_lshl_b32 s29, s31, 11
	v_add_u32 v5, s29, v5
	s_and_b32 s53, s25, 7
	v_lshlrev_b32 v10, 3, v2
	v_lshl_add_u32 v10, v1, 1, v10
	s_mov_b32 s92, 0x04000c0c
	s_mov_b32 s93, 0x0706000c
	v_lshlrev_b32 v11, 3, v1
	v_add_u32 v11, 13184, v11
	s_lshl_b32 s29, s53, 13
	s_lshl_b32 s30, s31, 8
	s_add_u32 s29, s29, s30
	v_lshl_add_u32 v8, v2, 6, s29
	v_lshl_add_u32 v8, v1, 4, v8
	s_add_u32 s54, s22, 921600
	s_addc_u32 s55, s23, 0
	s_barrier
	s_barrier
	s_barrier
	s_barrier
	global_load_dwordx4 v[12:15], v5, s[34:35] sc1
	global_load_dwordx4 v[16:19], v5, s[34:35] offset:1024 sc1
	s_barrier
	s_barrier
	s_branch .Lck4
.Lpad14:
	s_branch .Lpr14
.Lrt5:
	global_load_dwordx4 v[12:15], v5, s[34:35] sc1
)ASM"
R"ASM(	global_load_dwordx4 v[16:19], v5, s[34:35] offset:1024 sc1
	s_sub_u32 s33, s33, 1
	s_cbranch_scc1 .Ldn6
.Lck4:
	s_waitcnt vmcnt(0)
.Lck4b:
	v_bitop3_b32 v20, v12, v13, v14 bitop3:0x96
	v_bitop3_b32 v21, v16, v17, v18 bitop3:0x96
	v_xor_b32 v20, v20, v15
	v_xor_b32 v21, v21, v19
	v_bitop3_b32 v20, v20, v21, s26 bitop3:0x7e
	s_nop 1
	v_or_b32_dpp v20, v20, v20 quad_perm:[1,0,3,2] row_mask:0xf bank_mask:0xf
	v_cmpx_ne_u32 vcc, 0, v20
	s_cbranch_execnz .Lrt5
.Ldn6:
	s_mov_b64 exec, s[36:37]
	v_perm_b32 v20, v14, v13, s92
	v_perm_b32 v15, v20, v12, s93
	v_and_b32 v12, 0xffffff00, v12
	v_and_b32 v13, 0xffffff00, v13
	v_and_b32 v14, 0xffffff00, v14
	v_perm_b32 v21, v18, v17, s92
	v_perm_b32 v19, v21, v16, s93
	v_and_b32 v16, 0xffffff00, v16
	v_and_b32 v17, 0xffffff00, v17
	v_and_b32 v18, 0xffffff00, v18
	v_pk_add_f32 v[12:13], v[12:13], v[16:17]
	v_pk_add_f32 v[14:15], v[14:15], v[18:19]
	s_nop 1
	v_permlane32_swap_b32 v12, v14
	v_permlane32_swap_b32 v13, v15
	v_add_f32 v12, v12, v14
	v_add_f32 v14, v13, v15
	s_nop 1
	v_permlane16_swap_b32 v12, v14
	v_cvt_pk_bf16_f32 v12, v12, v14
	v_and_b32 v14, 0xffff0000, v12
	v_lshlrev_b32 v12, 16, v12
	v_mul_f32 v13, v12, v12
	v_mul_f32 v13, 0x3d372713, v13
	v_fma_f32 v13, v13, v12, v12
	v_mul_f32 v13, 0xc0135761, v13
	v_exp_f32 v13, v13
	s_nop 0
	v_add_f32 v13, 1.0, v13
	v_rcp_f32 v13, v13
	s_nop 0
	v_mul_f32 v12, v12, v13
	v_cvt_pk_bf16_f32 v12, v12, v12
	v_lshlrev_b32 v12, 16, v12
	v_mul_f32 v12, v12, v14
	v_cvt_pk_bf16_f32 v12, v12, v12
	ds_write_b16 v10, v12 offset:13184
	ds_read2_b64 v[16:19], v11 offset0:0 offset1:4
	ds_read2_b64 v[20:23], v11 offset0:8 offset1:12
	s_barrier
	ds_read_b64_tr_b16 v[32:33], v68 offset:0
	ds_read_b64_tr_b16 v[34:35], v68 offset:2048
	ds_read_b64_tr_b16 v[36:37], v68 offset:4096
	ds_read_b64_tr_b16 v[38:39], v68 offset:6144
	ds_read_b64_tr_b16 v[40:41], v69 offset:0
	ds_read_b64_tr_b16 v[42:43], v69 offset:2048
	ds_read_b64_tr_b16 v[44:45], v69 offset:4096
	ds_read_b64_tr_b16 v[46:47], v69 offset:6144
	ds_read_b64_tr_b16 v[48:49], v70 offset:0
	ds_read_b64_tr_b16 v[50:51], v70 offset:2048
	ds_read_b64_tr_b16 v[52:53], v70 offset:4096
	ds_read_b64_tr_b16 v[54:55], v70 offset:6144
	ds_read_b64_tr_b16 v[56:57], v71 offset:0
	ds_read_b64_tr_b16 v[58:59], v71 offset:2048
	ds_read_b64_tr_b16 v[60:61], v71 offset:4096
	ds_read_b64_tr_b16 v[62:63], v71 offset:6144
	s_waitcnt lgkmcnt(0)
	v_mfma_f32_16x16x32_bf16 v[72:75], v[32:35], v[16:19], 0
	v_mfma_f32_16x16x32_bf16 v[76:79], v[40:43], v[16:19], 0
	v_mfma_f32_16x16x32_bf16 v[80:83], v[48:51], v[16:19], 0
	v_mfma_f32_16x16x32_bf16 v[84:87], v[56:59], v[16:19], 0
	v_mfma_f32_16x16x32_bf16 v[72:75], v[36:39], v[20:23], v[72:75]
	v_mfma_f32_16x16x32_bf16 v[76:79], v[44:47], v[20:23], v[76:79]
	v_mfma_f32_16x16x32_bf16 v[80:83], v[52:55], v[20:23], v[80:83]
	v_mfma_f32_16x16x32_bf16 v[84:87], v[60:63], v[20:23], v[84:87]
	s_mov_b32 s92, 0x07060501
	s_mov_b32 s93, 0x07060502
	s_mov_b32 s94, 0x07060503
	s_nop 4
	v_cndmask_b32 v72, v72, v76, s[40:41]
	v_cndmask_b32 v73, v73, v77, s[40:41]
	v_cndmask_b32 v74, v74, v78, s[40:41]
	v_cndmask_b32 v75, v75, v79, s[40:41]
	v_cndmask_b32 v72, v72, v80, s[44:45]
	v_cndmask_b32 v73, v73, v81, s[44:45]
	v_cndmask_b32 v74, v74, v82, s[44:45]
	v_cndmask_b32 v75, v75, v83, s[44:45]
	v_cndmask_b32 v72, v72, v84, s[90:91]
	v_cndmask_b32 v73, v73, v85, s[90:91]
	v_cndmask_b32 v74, v74, v86, s[90:91]
	v_cndmask_b32 v75, v75, v87, s[90:91]
	v_add_u32 v72, 0x80, v72
	v_add_u32 v73, 0x80, v73
	v_add_u32 v74, 0x80, v74
	v_add_u32 v75, 0x80, v75
	v_perm_b32 v72, v72, v75, s92
	v_perm_b32 v73, v73, v75, s93
	v_perm_b32 v74, v74, v75, s94
	v_bitop3_b32 v75, v72, v73, v74 bitop3:0x96
	v_xor_b32 v75, s26, v75
	s_mov_b32 exec_lo, 0x000f000f
	s_mov_b32 exec_hi, 0x000f000f
	global_store_dwordx4 v8, v[72:75], s[54:55] sc1
.Laux_tail:
	s_barrier
	s_barrier
.Lexit_aux:
	s_endpgm
.Laux_idle:
	s_barrier
	s_barrier
	s_barrier
	s_barrier
	s_barrier
	s_barrier
	s_barrier
	s_branch .Laux_tail
.Lpad16:
	s_branch .Lpr16
; wave 0 of workgroups 0..31: g = silu(sum f), out = x + (sum s) * g for 16 columns
.Lfinal:
	ds_read_b32 v20, v9 offset:12
	s_waitcnt lgkmcnt(0)
	v_readfirstlane_b32 s26, v20
	s_mov_b64 s[36:37], -1
	s_mov_b32 s33, 0x40000
	s_mov_b32 s44, 0xbfb8aa3b
	v_and_b32 v6, 3, v0
	v_lshlrev_b32 v2, 3, v6
	v_lshl_add_u32 v2, v1, 1, v2
	s_lshl_b32 s29, s2, 5
	v_add_u32 v2, s29, v2
	s_lshr_b32 s29, s2, 2
	s_lshl_b32 s29, s29, 13
	s_and_b32 s30, s2, 3
	s_lshl_b32 s30, s30, 6
	s_add_u32 s29, s29, s30
	v_lshrrev_b32 v4, 2, v0
	v_lshlrev_b32 v4, 8, v4
	v_lshl_add_u32 v4, v6, 4, v4
	v_add_u32 v4, s29, v4
	v_cmp_gt_u32 vcc, 48, v0
	v_add_u32 v6, 4096, v4
	s_mov_b32 s92, 0x04000c0c
	s_mov_b32 s93, 0x0706000c
	v_cndmask_b32 v5, v4, v6, vcc
	v_cndmask_b32 v7, 0, 1.0, vcc
	v_mov_b32 v9, 0
	s_add_u32 s34, s22, 921600
	s_addc_u32 s35, s23, 0
	s_add_u32 s54, s22, 790528
	s_addc_u32 s55, s23, 0
	s_mov_b32 s38, 0x000f000f
	s_mov_b32 s39, s38
	s_waitcnt vmcnt(0)
	s_barrier
	global_load_ushort v3, v2, s[4:5]
	s_barrier
	global_load_dwordx4 v[12:15], v4, s[34:35] sc1
	global_load_dwordx4 v[16:19], v5, s[34:35] sc1
	s_branch .Lck7
.Lrt8:
	global_load_dwordx4 v[12:15], v4, s[34:35] sc1
	global_load_dwordx4 v[16:19], v5, s[34:35] sc1
	s_sub_u32 s33, s33, 1
	s_cbranch_scc1 .Ldn9
.Lck7:
	s_waitcnt vmcnt(0)
.Lck7b:
	v_bitop3_b32 v20, v12, v13, v14 bitop3:0x96
	v_bitop3_b32 v21, v16, v17, v18 bitop3:0x96
	v_xor_b32 v20, v20, v15
	v_xor_b32 v21, v21, v19
	v_bitop3_b32 v20, v20, v21, s26 bitop3:0x7e
	s_nop 1
	v_or_b32_dpp v20, v20, v20 quad_perm:[1,0,3,2] row_mask:0xf bank_mask:0xf
	v_cmpx_ne_u32 vcc, 0, v20
	s_cbranch_execnz .Lrt8
.Ldn9:
	s_mov_b64 exec, s[36:37]
	v_perm_b32 v20, v14, v13, s92
	v_perm_b32 v15, v20, v12, s93
	v_and_b32 v12, 0xffffff00, v12
	v_and_b32 v13, 0xffffff00, v13
	v_and_b32 v14, 0xffffff00, v14
	v_perm_b32 v21, v18, v17, s92
	v_perm_b32 v19, v21, v16, s93
	v_and_b32 v16, 0xffffff00, v16
	v_and_b32 v17, 0xffffff00, v17
	v_and_b32 v18, 0xffffff00, v18
	v_fmac_f32 v12, v16, v7
	v_fmac_f32 v13, v17, v7
	v_fmac_f32 v14, v18, v7
	v_fmac_f32 v15, v19, v7
	s_nop 0
	v_permlane32_swap_b32 v12, v14
	v_permlane32_swap_b32 v13, v15
	v_add_f32 v12, v12, v14
	v_add_f32 v13, v13, v15
	s_nop 1
	v_permlane16_swap_b32 v12, v13
	v_add_f32 v12, v12, v13
	s_nop 1
	v_add_f32_dpp v12, v12, v12 row_ror:4 row_mask:0xf bank_mask:0xf
	s_nop 1
	v_add_f32_dpp v12, v12, v12 row_ror:8 row_mask:0xf bank_mask:0xf
	v_cvt_pk_bf16_f32 v12, v12, v12
	v_lshlrev_b32 v12, 16, v12
	v_mul_f32 v13, s44, v12
	v_exp_f32 v13, v13
	s_nop 0
	v_add_f32 v13, 1.0, v13
	v_rcp_f32 v13, v13
	s_nop 0
	v_mul_f32 v8, v12, v13
	v_cvt_pk_bf16_f32 v8, v8, v8
	v_lshlrev_b32 v8, 16, v8
	v_lshlrev_b32 v3, 16, v3
	v_and_b32 v30, 31, v2
	v_mul_u32_u24 v30, 72, v30
	v_bfe_u32 v31, v0, 2, 2
	v_lshl_add_u32 v30, v31, 5, v30
	s_barrier
	ds_read_b128 v[32:35], v30 offset:6144
	ds_read_b128 v[36:39], v30 offset:6160
	s_waitcnt lgkmcnt(1)
	v_pk_add_f32 v[32:33], v[32:33], v[34:35]
	s_waitcnt lgkmcnt(0)
	v_pk_add_f32 v[36:37], v[36:37], v[38:39]
	v_pk_add_f32 v[32:33], v[32:33], v[36:37]
	v_add_f32 v52, v32, v33
	s_nop 1
	v_add_f32_dpp v52, v52, v52 row_ror:8 row_mask:0xf bank_mask:0xf
	s_nop 1
	v_add_f32_dpp v52, v52, v52 row_ror:4 row_mask:0xf bank_mask:0xf
	v_cvt_pk_bf16_f32 v52, v52, v52
	v_lshlrev_b32 v52, 16, v52
	v_mul_f32 v52, v52, v8
	v_cvt_pk_bf16_f32 v52, v52, v52
	v_lshlrev_b32 v52, 16, v52
	v_add_f32 v52, v52, v3
	v_cvt_pk_bf16_f32 v52, v52, v52
	s_mov_b64 exec, s[38:39]
	global_store_short v2, v52, s[20:21] sc0 sc1
.Lfin_end:
	s_endpgm
.Lend:
)ASM"
  : "+{v0}"(tid), "+{s2}"(bid)
  : "{s[0:1]}"(__builtin_amdgcn_kernarg_segment_ptr()), "n"(0), "s"((unsigned)(size_t)(__attribute__((address_space(3))) char*)lds)
  : "v1", "v2", "v3", "v4", "v5", "v6", "v7", "v8", "v9", "v10", "v11", "v12", "v13", "v14", "v15", "v16", "v17", "v18", "v19", "v20", "v21", "v22", "v23", "v24", "v25", "v26", "v27", "v28", "v29", "v30", "v31", "v32", "v33", "v34", "v35", "v36", "v37", "v38", "v39", "v40", "v41", "v42", "v43", "v44", "v45", "v46", "v47", "v48", "v49", "v50", "v51", "v52", "v53", "v54", "v55", "v56", "v57", "v58", "v59", "v60", "v61", "v62", "v63", "v64", "v65", "v66", "v67", "v68", "v69", "v70", "v71", "v72", "v73", "v74", "v75", "v76", "v77", "v78", "v79", "v80", "v81", "v82", "v83", "v84", "v85", "v86", "v87", "v88", "v89", "v90", "v91", "v92", "v93", "v94", "v95", "v96", "v97", "v98", "v99", "v100", "v101", "v102", "v103", "s3", "s4", "s5", "s6", "s7", "s8", "s9", "s10", "s11", "s12", "s13", "s14", "s15", "s16", "s17", "s18", "s19", "s20", "s21", "s22", "s23", "s24", "s25", "s26", "s27", "s28", "s29", "s30", "s31", "s33", "s34", "s35", "s36", "s37", "s38", "s39", "s40", "s41", "s42", "s43", "s44", "s45", "s46", "s47", "s48", "s49", "s50", "s51", "s52", "s53", "s54", "s55", "s90", "s91", "s92", "s93", "s94", "s95", "vcc", "scc", "memory");
#endif
}

extern "C" void kernel_launch(void* const* d_in, const int* in_sizes, int n_in, void* d_out, int out_size, void* d_ws, size_t ws_size,
                              hipStream_t stream) {
  fused_decode<<<256, 896, 0, stream>>>(d_in[0], d_in[1], d_in[2], d_in[3], d_in[4], d_in[6], d_in[7], d_in[8], d_out, d_ws);
}
```
